```python
import torch

def _softmax(x, axis=-1):
    return torch.softmax(x, dim=axis)
def _gelu(x):
    return torch.nn.functional.gelu(x, approximate="tanh")
def _silu(x):
    return torch.nn.functional.silu(x)
def _sigmoid(x):
    return torch.sigmoid(x)
def _layernorm(x, axis=-1, eps=1e-5):
    m = x.mean(dim=axis, keepdim=True)
    v = x.var(dim=axis, keepdim=True, unbiased=False)
    return (x - m) / torch.sqrt(v + eps)
def _scatter_add(tgt, idx, src, axis=0):
    return tgt.clone().index_add_(axis, idx, src)
def _segment_sum(src, seg, n):
    out = torch.zeros((n,) + src.shape[1:], dtype=src.dtype)
    return out.index_add_(0, seg, src)
def _pad(x, pw):
    flat = tuple(v for pair in reversed(pw) for v in pair)
    return torch.nn.functional.pad(x, flat)

kernel_name = 'sequential_preLN__compressed_kv_cla__glu_gated_reglu__decode'

def setup_inputs():

    _w = (lambda a: (lambda t: t.to(torch.bfloat16) if t.is_floating_point() else t)(torch.as_tensor(a)))
    def _A_setup():
        B, H, Sq, Sk, D, M = 1, 32, 1, 4096, 128, 640
        Dc = 4 * D
        return {
            "x": _w((lambda *s: torch.randn(*s, dtype=torch.bfloat16) * 0.05)(B, Sq, M)),
            "c_kv": _w((lambda *s: torch.randn(*s, dtype=torch.bfloat16) * 0.8)(B, Sk, Dc)),
            "w_q": _w((lambda *s: torch.randn(*s, dtype=torch.bfloat16) * 1.6)(M, H, D) * (M ** -0.5)),
            "w_uk": _w((lambda *s: torch.randn(*s, dtype=torch.bfloat16) * 1.6)(Dc, H, D) * (Dc ** -0.5)),
            "w_uv": _w((lambda *s: torch.randn(*s, dtype=torch.bfloat16) * 0.8)(Dc, H, D) * (Dc ** -0.5)),
            "w_o": _w((lambda *s: torch.randn(*s, dtype=torch.bfloat16) * 0.8)(H, D, M) * ((H * D) ** -0.5)),
        }
    _A_in = _A_setup()

    def _B_setup():
        B, S, D, F = 1, 1, 640, 1728
        return {
            "x": _w((lambda *s: torch.randn(*s, dtype=torch.bfloat16) * 0.05)(B, S, D)),
            "W_gate": _w((lambda *s: torch.randn(*s, dtype=torch.bfloat16) * 0.4)(D, F) * (2.0 / D) ** 0.5),
            "W_up": _w((lambda *s: torch.randn(*s, dtype=torch.bfloat16) * 0.4)(D, F) * (2.0 / D) ** 0.5),
            "W_down": _w((lambda *s: torch.randn(*s, dtype=torch.bfloat16) * 0.4)(F, D) * (1.0 / F) ** 0.5),
        }
    _B_in = _B_setup()

    return {
        "A_x": _A_in["x"],
        "A_c_kv": _A_in["c_kv"],
        "A_w_q": _A_in["w_q"],
        "A_w_uk": _A_in["w_uk"],
        "A_w_uv": _A_in["w_uv"],
        "A_w_o": _A_in["w_o"],
        "B_x": _B_in["x"],
        "B_W_gate": _B_in["W_gate"],
        "B_W_up": _B_in["W_up"],
        "B_W_down": _B_in["W_down"],
    }

def reference(A_x, A_c_kv, A_w_q, A_w_uk, A_w_uv, A_w_o, B_x, B_W_gate, B_W_up, B_W_down):
    def _A_ref(x, c_kv, w_q, w_uk, w_uv, w_o):
        B, Sq, M = x.shape[0], x.shape[1], x.shape[2]
        Sk = c_kv.shape[1]
        H, D = w_q.shape[1], w_q.shape[2]
        q = torch.einsum("bqm,mhd->bhqd", x, w_q)
        k = torch.einsum("bkc,chd->bhkd", c_kv, w_uk)
        v = torch.einsum("bkc,chd->bhkd", c_kv, w_uv)
        scores = torch.einsum("bhqd,bhkd->bhqk", q, k) * (D ** -0.5)
        off = Sk - Sq
        qi = torch.arange(Sq)[:, None]
        kj = torch.arange(Sk)[None, :]
        cmask = kj <= (qi + off)
        scores = torch.where(cmask[None, None], scores, -1e9)
        p = _softmax(scores, axis=-1)
        ctx = torch.einsum("bhqk,bhkd->bhqd", p, v)
        return torch.einsum("bhqd,hdm->bqm", ctx, w_o)

    def _B_ref(x, W_gate, W_up, W_down):
        gate = torch.einsum("bsd,df->bsf", x, W_gate)
        up = torch.einsum("bsd,df->bsf", x, W_up)
        gate = gate * (lambda x, dt: x.to(dt))(gate > 0, gate.dtype)
        h = gate * up
        return torch.einsum("bsf,fd->bsd", h, W_down)

    x = A_x
    h = x + _A_ref(_layernorm(x, axis=-1), A_c_kv, A_w_q, A_w_uk, A_w_uv, A_w_o)
    h2 = h + _B_ref(_layernorm(h, axis=-1), B_W_gate, B_W_up, B_W_down)
    return h2
```

```cpp
#include <hip/hip_runtime.h>
#include <hip/hip_bf16.h>

typedef unsigned short u16;
__device__ __forceinline__ float bf2f(u16 b) { return __uint_as_float(((unsigned)b) << 16); }
__device__ __forceinline__ u16 f2bf(float f) { unsigned u = __float_as_uint(f); u += 0x7FFF + ((u >> 16) & 1); return (u16)(u >> 16); }
__device__ __forceinline__ float rbf(float f) { return bf2f(f2bf(f)); }

constexpr int H = 32, SK = 4096, D = 128, M = 640, DC = 512, F = 1728;

__device__ float block_sum(float v, float* red) {
  for (int o = 32; o >= 1; o >>= 1) v += __shfl_xor(v, o);
  int w = threadIdx.x >> 6, l = threadIdx.x & 63;
  __syncthreads();
  if (l == 0) red[w] = v;
  __syncthreads();
  float s = 0;
  for (int i = 0; i < (int)(blockDim.x >> 6); ++i) s += red[i];
  return s;
}

__device__ void layernorm(const float* src, float* xn, float* red) {
  float s = 0;
  for (int i = threadIdx.x; i < M; i += blockDim.x) s += src[i];
  float mean = block_sum(s, red) / M;
  float v = 0;
  for (int i = threadIdx.x; i < M; i += blockDim.x) { float d = src[i] - mean; v += d * d; }
  float var = block_sum(v, red) / M;
  float rs = 1.0f / sqrtf(var + 1e-5f);
  for (int i = threadIdx.x; i < M; i += blockDim.x) xn[i] = rbf((src[i] - mean) * rs);
  __syncthreads();
}

__global__ void k_q(const u16* x, const u16* wq, float* q) {
  __shared__ float xs[M], xn[M], red[16];
  for (int i = threadIdx.x; i < M; i += blockDim.x) xs[i] = bf2f(x[i]);
  __syncthreads();
  layernorm(xs, xn, red);
  int col = blockIdx.x * blockDim.x + threadIdx.x;
  float acc = 0;
  for (int m = 0; m < M; ++m) acc += xn[m] * bf2f(wq[(size_t)m * (H * D) + col]);
  q[col] = rbf(acc);
}
__global__ void k_qa(const float* q, const u16* wuk, float* qa) {
  int idx = blockIdx.x * blockDim.x + threadIdx.x;
  int h = idx / DC, c = idx % DC;
  float acc = 0;
  for (int d = 0; d < D; ++d) acc += q[h * D + d] * bf2f(wuk[((size_t)c * H + h) * D + d]);
  qa[idx] = acc;
}
__global__ void k_scores(const float* qa, const u16* ckv, float* sc) {
  int idx = blockIdx.x * blockDim.x + threadIdx.x;
  int h = idx / SK, k = idx % SK;
  float acc = 0;
  for (int c = 0; c < DC; ++c) acc += qa[h * DC + c] * bf2f(ckv[(size_t)k * DC + c]);
  sc[idx] = acc * 0.08838834764831845f;
}
__global__ void k_softmax(float* sc) {
  __shared__ float red[16];
  int h = blockIdx.x;
  float m = -1e30f;
  for (int k = threadIdx.x; k < SK; k += blockDim.x) m = fmaxf(m, sc[h * SK + k]);
  for (int o = 32; o >= 1; o >>= 1) m = fmaxf(m, __shfl_xor(m, o));
  if ((threadIdx.x & 63) == 0) red[threadIdx.x >> 6] = m;
  __syncthreads();
  for (int i = 0; i < (int)(blockDim.x >> 6); ++i) m = fmaxf(m, red[i]);
  float s = 0;
  for (int k = threadIdx.x; k < SK; k += blockDim.x) { float e = __expf(sc[h * SK + k] - m); sc[h * SK + k] = e; s += e; }
  s = block_sum(s, red);
  for (int k = threadIdx.x; k < SK; k += blockDim.x) sc[h * SK + k] /= s;
}
__global__ void k_ctxc(const float* p, const u16* ckv, float* cc) {
  int idx = blockIdx.x * blockDim.x + threadIdx.x;
  int h = idx / DC, c = idx % DC;
  float acc = 0;
  for (int k = 0; k < SK; ++k) acc += p[h * SK + k] * bf2f(ckv[(size_t)k * DC + c]);
  cc[idx] = acc;
}
__global__ void k_ctx(const float* cc, const u16* wuv, float* ctx) {
  int idx = blockIdx.x * blockDim.x + threadIdx.x;
  int h = idx / D, d = idx % D;
  float acc = 0;
  for (int c = 0; c < DC; ++c) acc += cc[h * DC + c] * bf2f(wuv[((size_t)c * H + h) * D + d]);
  ctx[idx] = rbf(acc);
}
__global__ void k_o(const float* ctx, const u16* wo, const u16* x, float* hh) {
  int m = blockIdx.x * blockDim.x + threadIdx.x;
  float acc = 0;
  for (int i = 0; i < H * D; ++i) acc += ctx[i] * bf2f(wo[(size_t)i * M + m]);
  hh[m] = rbf(bf2f(x[m]) + rbf(acc));
}
__global__ void k_gu(const float* hh, const u16* wg, const u16* wu, float* g) {
  __shared__ float xn[M], red[16];
  layernorm(hh, xn, red);
  int f = blockIdx.x * blockDim.x + threadIdx.x;
  if (f >= F) return;
  float a = 0, b = 0;
  for (int m = 0; m < M; ++m) { a += xn[m] * bf2f(wg[(size_t)m * F + f]); b += xn[m] * bf2f(wu[(size_t)m * F + f]); }
  a = rbf(a); b = rbf(b);
  g[f] = rbf((a > 0 ? a : 0.0f * a) * b);
}
__global__ void k_down(const float* g, const u16* wd, const float* hh, u16* out) {
  int m = blockIdx.x * blockDim.x + threadIdx.x;
  float acc = 0;
  for (int f = 0; f < F; ++f) acc += g[f] * bf2f(wd[(size_t)f * M + m]);
  out[m] = f2bf(hh[m] + rbf(acc));
}

extern "C" void kernel_launch(void* const* d_in, const int* in_sizes, int n_in, void* d_out, int out_size, void* d_ws,
                              size_t ws_size, hipStream_t stream) {
  const u16* x = (const u16*)d_in[0];
  const u16* ckv = (const u16*)d_in[1];
  const u16* wq = (const u16*)d_in[2];
  const u16* wuk = (const u16*)d_in[3];
  const u16* wuv = (const u16*)d_in[4];
  const u16* wo = (const u16*)d_in[5];
  const u16* wg = (const u16*)d_in[7];
  const u16* wu = (const u16*)d_in[8];
  const u16* wd = (const u16*)d_in[9];
  float* ws = (float*)d_ws;
  float* q = ws; float* qa = q + 4096; float* sc = qa + 16384; float* cc = sc + H * SK; float* ctx = cc + 16384;
  float* hh = ctx + 4096; float* g = hh + 1024;
  k_q<<<16, 256, 0, stream>>>(x, wq, q);
  k_qa<<<64, 256, 0, stream>>>(q, wuk, qa);
  k_scores<<<512, 256, 0, stream>>>(qa, ckv, sc);
  k_softmax<<<32, 256, 0, stream>>>(sc);
  k_ctxc<<<64, 256, 0, stream>>>(sc, ckv, cc);
  k_ctx<<<16, 256, 0, stream>>>(cc, wuv, ctx);
  k_o<<<10, 64, 0, stream>>>(ctx, wo, x, hh);
  k_gu<<<7, 256, 0, stream>>>(hh, wg, wu, g);
  k_down<<<10, 64, 0, stream>>>(g, wd, hh, (u16*)d_out);
}
```

```cpp
#include <hip/hip_runtime.h>

typedef unsigned short u16;
typedef unsigned u32;
typedef __attribute__((ext_vector_type(4))) u32 u32x4;
typedef __attribute__((ext_vector_type(2))) u32 u32x2;
typedef __attribute__((ext_vector_type(4))) float f32x4;
typedef __attribute__((ext_vector_type(2))) float f32x2;
typedef __attribute__((ext_vector_type(8))) __bf16 bf16x8;
typedef __attribute__((ext_vector_type(2))) __bf16 bf16x2;
typedef __attribute__((ext_vector_type(4))) short s16x4;
typedef __amdgpu_buffer_rsrc_t rsrc_t;
#define DI __device__ __forceinline__
#define LDSP __attribute__((address_space(3)))

constexpr int SPIN_MAX = 200000;
constexpr u32 OFF_A = 4096;
constexpr u32 OFF_B = OFF_A + 32 * 4 * 64 * 16;
constexpr u32 OFF_ML = OFF_B + 32 * 128 * 16;
constexpr u32 OFF_D = OFF_ML + 32 * 32 * 16;
constexpr u32 OFF_E = OFF_D + 8 * 16 * 32 * 16;
constexpr u32 OFF_G = OFF_E + 8 * 160 * 16;
constexpr u32 OFF_C = OFF_G + 40 * 108 * 4 * 16;
constexpr u32 WS_USED = OFF_C + 32 * 4 * 32 * 32 * 16;
constexpr u32 OFF_DBG = 8u << 20;

constexpr int L_BIG = 0;
constexpr int L_WUV = 0, L_WG = 32768, L_WU = 32768 + 20480;
constexpr int L_SM = 133120;
constexpr int L_QA = L_SM;
constexpr int L_P = L_QA + 8 * 1056;
constexpr int L_MX = L_P + 8 * 288;
constexpr int L_CTX = L_MX + 256;
constexpr int L_RED = L_SM;
constexpr int L_Q = L_RED + 4096;
constexpr int L_CC = L_Q + 256;
constexpr int L_H = L_CC + 256;
constexpr int L_GU = L_H + 1280;
constexpr int L_TOTAL = L_SM + 14336;

DI float bf2f(u32 b) { return __uint_as_float(b << 16); }
DI float bfhi(u32 w) { return __uint_as_float(w & 0xffff0000u); }
DI float bflo(u32 w) { return __uint_as_float(w << 16); }
DI u32 pack2(float lo, float hi) { f32x2 v = {lo, hi}; return __builtin_bit_cast(u32, __builtin_convertvector(v, bf16x2)); }
DI float rbf(float f) { return bflo(pack2(f, f)); }
DI void bar() { asm volatile("s_waitcnt lgkmcnt(0)\n\ts_barrier" ::: "memory"); }
#define FENCE() do { asm volatile("" ::: "memory"); __builtin_amdgcn_sched_barrier(0); } while (0)
DI void vm0bar() { asm volatile("s_waitcnt vmcnt(0) lgkmcnt(0)\n\ts_barrier" ::: "memory"); }
template <int C> DI float dpp(float v) { return __builtin_bit_cast(float, __builtin_amdgcn_update_dpp(0, __builtin_bit_cast(int, v), C, 0xf, 0xf, true)); }
constexpr int DPP_X1 = 0xB1, DPP_X2 = 0x4E, DPP_ROR4 = 0x124, DPP_ROR8 = 0x128;
DI void swap32(float& a, float& b) { auto r = __builtin_amdgcn_permlane32_swap(__float_as_uint(a), __float_as_uint(b), false, false); const u32 r0 = r[0], r1 = r[1]; a = __uint_as_float(r0); b = __uint_as_float(r1); }
DI void swap16(float& a, float& b) { auto r = __builtin_amdgcn_permlane16_swap(__float_as_uint(a), __float_as_uint(b), false, false); const u32 r0 = r[0], r1 = r[1]; a = __uint_as_float(r0); b = __uint_as_float(r1); }
DI float xsum16(float v) { float a = v, b = v; swap16(a, b); return a + b; }
DI float xsum32(float v) { float a = v, b = v; swap32(a, b); return a + b; }
DI float xmax16(float v) { float a = v, b = v; swap16(a, b); return fmaxf(a, b); }
DI float xmax32(float v) { float a = v, b = v; swap32(a, b); return fmaxf(a, b); }
DI float wave_sum(float v) { v += dpp<DPP_X1>(v); v += dpp<DPP_X2>(v); v += dpp<DPP_ROR4>(v); v += dpp<DPP_ROR8>(v); return xsum32(xsum16(v)); }
DI float reduce_a(float v0, float v1, float v2, float v3) {
  swap32(v0, v2); float X = v0 + v2;
  swap32(v1, v3); float Y = v1 + v3;
  swap16(X, Y); float Z = X + Y;
  Z += dpp<DPP_ROR4>(Z); Z += dpp<DPP_ROR8>(Z);
  return Z;
}
DI f32x4 mfma(u32x4 a, u32x4 b, f32x4 c) { return __builtin_amdgcn_mfma_f32_16x16x32_bf16(__builtin_bit_cast(bf16x8, a), __builtin_bit_cast(bf16x8, b), c, 0, 0, 0); }
DI rsrc_t mkrsrc(const void* p, u32 bytes) { return __builtin_amdgcn_make_buffer_rsrc((void*)p, 0, bytes, 0x00020000); }
DI u32x4 ld16(rsrc_t r, u32 off) { return __builtin_amdgcn_raw_buffer_load_b128(r, off, 0, 0); }
DI u32x2 ld8(rsrc_t r, u32 off) { return __builtin_amdgcn_raw_buffer_load_b64(r, off, 0, 0); }
DI u32x4 ld16c(rsrc_t r, u32 off) { return __builtin_amdgcn_raw_buffer_load_b128(r, off, 0, 16); }
DI void st16c(rsrc_t r, u32 off, u32x4 v) { __builtin_amdgcn_raw_buffer_store_b128(v, r, off, 0, 16); }
typedef __attribute__((ext_vector_type(4))) int srd_t;
DI srd_t mksrd(const void* p, u32 bytes) {
  unsigned long long a = (unsigned long long)p;
  srd_t s = {(int)(u32)a, (int)(u32)(a >> 32), (int)bytes, 0x00020000};
  return s;
}
DI void dma16(srd_t srd, u32 voff, u32 soff, u32 lds) {
  u32 keep;
  asm volatile("s_nop 4\n\ts_mov_b32 %0, m0\n\ts_mov_b32 m0, %1\n\ts_nop 0\n\tbuffer_load_dwordx4 %2, %3, %4 offen lds\n\ts_mov_b32 m0, %0"
               : "=&s"(keep) : "s"(lds), "v"(voff), "s"(srd), "s"(soff) : "memory");
}
DI u32x2 trread(const char* p) { return __builtin_bit_cast(u32x2, __builtin_amdgcn_ds_read_tr16_b64_v4i16((LDSP s16x4*)p)); }
DI u32x4 trfrag(const char* p, int rowstride) {
  u32x2 a = trread(p), b = trread(p + 4 * rowstride);
  u32x4 r = {a[0], a[1], b[0], b[1]};
  return r;
}
DI bool tagok(u32x4 g, u32 tag) { return g[1] == tag && g[3] == ~tag; }
template <int N> DI void poll(rsrc_t ws, const u32 (&off)[N], u32 tag, u32x4 (&g)[N]) {
  int it = SPIN_MAX;
  bool more;
#pragma nounroll
  do {
    u32 bad = 0;
#pragma unroll
    for (int i = 0; i < N; ++i) g[i] = ld16c(ws, off[i]);
#pragma unroll
    for (int i = 0; i < N; ++i) bad |= (g[i][1] ^ tag) | (g[i][3] ^ ~tag);
    more = __any(bad != 0) && --it > 0;
    asm volatile("" ::: "memory");
  } while (more);
}
DI u32x4 gran(u32 p0, u32 p1, u32 tag) { u32x4 g = {p0, tag, p1, ~tag}; return g; }
DI u32x4 gather4(float v, u32 tag) {
  float v1 = dpp<0x55>(v), v2 = dpp<0xAA>(v), v3 = dpp<0xFF>(v), v0 = dpp<0x00>(v);
  return gran(pack2(v0, v1), pack2(v2, v3), tag);
}
DI float ln_std(float var) { return rbf(__builtin_sqrtf(rbf(rbf(var) + 1e-5f))); }

#define STAMP(i)
#define FLUSH()

__global__ __launch_bounds__(512, 2) void mega(const u16* x, const u16* ckv, const u16* wq,
                                               const u16* wuk, const u16* wuv, const u16* wo,
                                               const u16* wg, const u16* wu, const u16* wd,
                                               u16* out, unsigned char* wsp) {
  __shared__ __attribute__((aligned(16))) char smem[L_TOTAL];
  const int tid = threadIdx.x, lane = tid & 63;
  const int wid = __builtin_amdgcn_readfirstlane(tid >> 6);
  const int xc = blockIdx.x & 7, l = blockIdx.x >> 3;
  const int g = lane >> 4, n = lane & 15;
  const u32 lds0 = (u32)(size_t)smem;
  rsrc_t ws = mkrsrc(wsp, WS_USED);
  rsrc_t rx = mkrsrc(x, 1280);
  STAMP(0);
  const u32 epoch_v = __builtin_amdgcn_raw_buffer_load_b32(ws, 0, 0, 16);

  if (l < 16) {
    const int h = 4 * xc + (l >> 2), j = l & 3;
    rsrc_t rwq = mkrsrc(wq, 640 * 4096 * 2), rwuk = mkrsrc(wuk, 512 * 4096 * 2), rwd = mkrsrc(wd, 1728 * 640 * 2);
    u32x4 xa = ld16(rx, lane * 16);
    u32 xb = __builtin_amdgcn_raw_buffer_load_b32(rx, 1024 + lane * 4, 0, 0);
    u32 xr[5];
#pragma unroll
    for (int s = 0; s < 5; ++s) xr[s] = __builtin_amdgcn_raw_buffer_load_b16(rx, (160 * j + 20 * wid + 4 * s + g) * 2, 0, 0);
    FENCE();
    u32x4 wqf[5], wkf[4];
#pragma unroll
    for (int s = 0; s < 5; ++s) wqf[s] = ld16(rwq, ((160 * j + 20 * wid + 4 * s + g) * 4096 + h * 128 + 8 * n) * 2);
    FENCE();
#pragma unroll
    for (int s = 0; s < 4; ++s) wkf[s] = ld16(rwuk, ((128 * j + 16 * wid + 4 * g + (n >> 2)) * 4096 + h * 128 + 32 * s + 8 * (n & 3)) * 2);
    FENCE();
    const u32 tag = __builtin_amdgcn_readfirstlane(epoch_v) + 1;
    float sum = bflo(xb) + bfhi(xb);
#pragma unroll
    for (int i = 0; i < 4; ++i) sum += bflo(xa[i]) + bfhi(xa[i]);
    const float mean = wave_sum(sum) * (1.0f / 640.0f);
    float sq = (bflo(xb) - mean) * (bflo(xb) - mean) + (bfhi(xb) - mean) * (bfhi(xb) - mean);
#pragma unroll
    for (int i = 0; i < 4; ++i) sq += (bflo(xa[i]) - mean) * (bflo(xa[i]) - mean) + (bfhi(xa[i]) - mean) * (bfhi(xa[i]) - mean);
    const float xstd = ln_std(wave_sum(sq) * (1.0f / 640.0f)), xmean = rbf(mean);
    STAMP(1);
    {
      f32x4 acc = {0, 0, 0, 0};
#pragma unroll
      for (int s = 0; s < 5; ++s) {
        float a = rbf(bf2f(xr[s]) - xmean) / xstd;
        u32 ab = pack2(a, a) & ((n & 1) ? 0xffff0000u : 0x0000ffffu);
        const int r = (n & 7) >> 1;
        u32x4 A = {r == 0 ? ab : 0u, r == 1 ? ab : 0u, r == 2 ? ab : 0u, r == 3 ? ab : 0u};
        acc = mfma(A, wqf[s], acc);
      }
      if (g < 2) *(LDSP f32x4*)(smem + L_RED + wid * 512 + (8 * n + 4 * g) * 4) = acc;
    }
    bar();
    STAMP(2);
    if (wid == 0) {
      f32x2 v = {0, 0};
#pragma unroll
      for (int w = 0; w < 8; ++w) v += *(LDSP f32x2*)(smem + L_RED + w * 512 + lane * 8);
      st16c(ws, OFF_A + ((h * 4 + j) * 64 + lane) * 16, gran(__float_as_uint(v[0]), __float_as_uint(v[1]), tag));
      STAMP(3);
      u32 off[4]; u32x4 gq[4];
#pragma unroll
      for (int i = 0; i < 4; ++i) off[i] = OFF_A + ((h * 4 + i) * 64 + lane) * 16;
      poll<4>(ws, off, tag, gq);
      float q0 = 0, q1 = 0;
#pragma unroll
      for (int i = 0; i < 4; ++i) { q0 += __uint_as_float(gq[i][0]); q1 += __uint_as_float(gq[i][2]); }
      *(LDSP u32*)(smem + L_Q + lane * 4) = pack2(q0, q1);
      STAMP(4);
    }
    bar();
    {
      f32x4 acc = {0, 0, 0, 0};
#pragma unroll
      for (int s = 0; s < 4; ++s) {
        u32x4 A = *(LDSP u32x4*)(smem + L_Q + (32 * s + 8 * (n & 3)) * 2);
        if (g != (n >> 2)) A = u32x4{0, 0, 0, 0};
        acc = mfma(A, wkf[s], acc);
      }
      const int r = n & 3;
      float v01 = (r & 1) ? acc[1] : acc[0], v23 = (r & 1) ? acc[3] : acc[2];
      float v = (r & 2) ? v23 : v01;
      v += dpp<DPP_X1>(v); v += dpp<DPP_X2>(v);
      float v1 = dpp<0x104>(v), v2 = dpp<0x108>(v), v3 = dpp<0x10C>(v);
      if (n == 0) st16c(ws, OFF_B + (h * 128 + 32 * j + 4 * wid + g) * 16, gran(pack2(v, v1), pack2(v2, v3), tag));
    }
    STAMP(5);
    const int fg = xc + 8 * (l >> 2);
    const bool ffn = fg < 27;
    const int f0 = 64 * fg + 16 * (l & 3);
    {
      srd_t s = mksrd(wuv, 512 * 4096 * 2);
#pragma unroll
      for (int u = 0; u < 4; ++u)
        dma16(s, (g * 4096 + n * 8) * 2, ((128 * j + 16 * wid + 4 * u) * 4096 + h * 128) * 2, lds0 + L_WUV + (16 * wid + 4 * u) * 256);
    }
    u32x2 wdr[8];
    if (ffn && wid < 5) {
#pragma unroll
      for (int e = 0; e < 8; ++e) wdr[e] = ld8(rwd, ((f0 + 4 * g + (e & 3)) * 640 + 64 * (2 * wid + (e >> 2)) + 4 * n) * 2);
    }
    vm0bar();
    u32x4 wvf[4];
#pragma unroll
    for (int s = 0; s < 4; ++s) wvf[s] = trfrag(smem + L_WUV + (32 * s + 8 * g + (n >> 2)) * 256 + (16 * wid + 4 * (n & 3)) * 2, 256);
    u32x4 wdf[4];
#pragma unroll
    for (int t = 0; t < 4; ++t) {
      const u32 sel = (t & 1) ? 0x07060302u : 0x05040100u;
      wdf[t][0] = __builtin_amdgcn_perm(wdr[1][t >> 1], wdr[0][t >> 1], sel);
      wdf[t][1] = __builtin_amdgcn_perm(wdr[3][t >> 1], wdr[2][t >> 1], sel);
      wdf[t][2] = __builtin_amdgcn_perm(wdr[5][t >> 1], wdr[4][t >> 1], sel);
      wdf[t][3] = __builtin_amdgcn_perm(wdr[7][t >> 1], wdr[6][t >> 1], sel);
    }
    STAMP(6);
    {
      const int a = lane >> 2, gq = lane & 3;
      u32 off[2]; u32x4 gm[2];
#pragma unroll
      for (int u = 0; u < 2; ++u) off[u] = OFF_ML + (h * 32 + a + 16 * u) * 16;
      poll<2>(ws, off, tag, gm);
      float m0 = __uint_as_float(gm[0][0]), m1 = __uint_as_float(gm[1][0]);
      float M = fmaxf(m0, m1);
      M = fmaxf(M, dpp<DPP_ROR4>(M)); M = fmaxf(M, dpp<DPP_ROR8>(M)); M = xmax32(xmax16(M));
      float s0 = __builtin_amdgcn_exp2f(m0 - M), s1 = __builtin_amdgcn_exp2f(m1 - M);
      float L = s0 * __uint_as_float(gm[0][2]) + s1 * __uint_as_float(gm[1][2]);
      L += dpp<DPP_ROR4>(L); L += dpp<DPP_ROR8>(L); L = xsum32(xsum16(L));
      const float rl = 1.0f / L; s0 *= rl; s1 *= rl;
      STAMP(7);
      u32x4 gc[2];
#pragma unroll
      for (int u = 0; u < 2; ++u) off[u] = OFF_C + (((h * 4 + j) * 32 + a + 16 * u) * 32 + 4 * wid + gq) * 16;
      poll<2>(ws, off, tag, gc);
      STAMP(8);
      float z = reduce_a(s0 * bflo(gc[0][0]) + s1 * bflo(gc[1][0]), s0 * bfhi(gc[0][0]) + s1 * bfhi(gc[1][0]),
                         s0 * bflo(gc[0][2]) + s1 * bflo(gc[1][2]), s0 * bfhi(gc[0][2]) + s1 * bfhi(gc[1][2]));
      if ((lane & 12) == 0) *(LDSP u16*)(smem + L_CC + (16 * wid + 4 * gq + g) * 2) = (u16)pack2(z, z);
    }
    bar();
    {
      f32x4 acc = {0, 0, 0, 0};
#pragma unroll
      for (int s = 0; s < 4; ++s) acc = mfma(*(LDSP u32x4*)(smem + L_CC + (32 * s + 8 * g) * 2), wvf[s], acc);
      u32x4 gr = gather4(acc[0], tag);
      if (lane < 16 && (lane & 3) == 0) st16c(ws, OFF_D + (((xc * 4 + (l >> 2)) * 4 + j) * 32 + 4 * wid + (lane >> 2)) * 16, gr);
    }
    STAMP(9);
    if (!ffn) { FLUSH(); return; }
    {
      srd_t sg = mksrd(wg, 640 * 1728 * 2), su = mksrd(wu, 640 * 1728 * 2);
#pragma unroll
      for (int u = 0; u < 5; ++u) {
        const int blk = wid * 5 + u;
        const u32 voff = ((lane >> 1) * 1728 + (lane & 1) * 8) * 2;
        if (blk < 20) dma16(sg, voff, (blk * 32 * 1728 + f0) * 2, lds0 + L_WG + blk * 1024);
        else dma16(su, voff, ((blk - 20) * 32 * 1728 + f0) * 2, lds0 + L_WU + (blk - 20) * 1024);
      }
    }
    u32x2 xres = ld8(rx, (32 * wid + (lane & 31)) * 8 < 1280 ? (32 * wid + (lane & 31)) * 8 : 0);
    vm0bar();
    u32x4 guf[10];
    const int mat = wid & 1, kh = (wid >> 1) & 1;
    if (wid < 4) {
      f32x4 cs = {0, 0, 0, 0};
      const u32x4 ones = {0x3f803f80u, 0x3f803f80u, 0x3f803f80u, 0x3f803f80u};
#pragma unroll
      for (int s = 0; s < 10; ++s) {
        guf[s] = trfrag(smem + (mat ? L_WU : L_WG) + (32 * (10 * kh + s) + 8 * g + (n >> 2)) * 32 + 8 * (n & 3), 32);
        cs = mfma(guf[s], ones, cs);
      }
      if (n == 0) *(LDSP f32x4*)(smem + L_GU + 256 + (mat * 2 + kh) * 64 + g * 16) = cs;
    }
    STAMP(10);
    if (wid < 5) {
      u32 off[4]; u32x4 ge[4];
#pragma unroll
      for (int u = 0; u < 4; ++u) off[u] = OFF_E + ((4 * (lane >> 5) + u) * 160 + 32 * wid + (lane & 31)) * 16;
      poll<4>(ws, off, tag, ge);
      float v0 = 0, v1 = 0, v2 = 0, v3 = 0;
#pragma unroll
      for (int u = 0; u < 4; ++u) { v0 += bflo(ge[u][0]); v1 += bfhi(ge[u][0]); v2 += bflo(ge[u][2]); v3 += bfhi(ge[u][2]); }
      swap32(v0, v2); swap32(v1, v3);
      float a0 = rbf(v0 + v2), a1 = rbf(v1 + v3);
      u32 xw = lane < 32 ? xres[0] : xres[1];
      *(LDSP u32*)(smem + L_H + (32 * wid + (lane & 31)) * 8 + (lane >> 5) * 4) = pack2(bflo(xw) + a0, bfhi(xw) + a1);
    }
    STAMP(11);
    bar();
    if (wid < 4) {
      f32x4 acc = {0, 0, 0, 0};
#pragma unroll
      for (int s = 0; s < 10; ++s) acc = mfma(guf[s], *(LDSP u32x4*)(smem + L_H + (32 * (10 * kh + s) + 8 * g) * 2), acc);
      if (n == 0) *(LDSP f32x4*)(smem + L_GU + (mat * 2 + kh) * 64 + g * 16) = acc;
    } else if (wid == 4) {
      u32 hv[5];
      float s1 = 0;
#pragma unroll
      for (int u = 0; u < 5; ++u) { hv[u] = *(LDSP u32*)(smem + L_H + (lane + 64 * u) * 4); s1 += bflo(hv[u]) + bfhi(hv[u]); }
      const float mean2 = wave_sum(s1) * (1.0f / 640.0f);
      float s2 = 0;
#pragma unroll
      for (int u = 0; u < 5; ++u) { float d0 = bflo(hv[u]) - mean2, d1 = bfhi(hv[u]) - mean2; s2 += d0 * d0 + d1 * d1; }
      const float sd = ln_std(wave_sum(s2) * (1.0f / 640.0f));
      if (lane == 0) { f32x2 st = {rbf(mean2), 1.0f / sd}; *(LDSP f32x2*)(smem + L_GU + 512) = st; }
    }
    bar();
    if (wid < 5) {
      const f32x2 st = *(LDSP f32x2*)(smem + L_GU + 512);
      f32x4 ga = *(LDSP f32x4*)(smem + L_GU + 0 * 64 + g * 16) + *(LDSP f32x4*)(smem + L_GU + 1 * 64 + g * 16);
      f32x4 ua = *(LDSP f32x4*)(smem + L_GU + 2 * 64 + g * 16) + *(LDSP f32x4*)(smem + L_GU + 3 * 64 + g * 16);
      f32x4 gc = *(LDSP f32x4*)(smem + L_GU + 256 + 0 * 64 + g * 16) + *(LDSP f32x4*)(smem + L_GU + 256 + 1 * 64 + g * 16);
      f32x4 uc = *(LDSP f32x4*)(smem + L_GU + 256 + 2 * 64 + g * 16) + *(LDSP f32x4*)(smem + L_GU + 256 + 3 * 64 + g * 16);
      float gv[4];
#pragma unroll
      for (int r = 0; r < 4; ++r) {
        float gt = rbf((ga[r] - st[0] * gc[r]) * st[1]), up = rbf((ua[r] - st[0] * uc[r]) * st[1]);
        gv[r] = (gt > 0.0f ? gt : 0.0f * gt) * up;
      }
      const u32 p01 = pack2(gv[0], gv[1]), p23 = pack2(gv[2], gv[3]);
      u32x4 A = n < 8 ? u32x4{p01, p23, 0, 0} : u32x4{0, 0, p01, p23};
      float o[4];
#pragma unroll
      for (int t = 0; t < 4; ++t) { f32x4 z = {0, 0, 0, 0}; z = mfma(A, wdf[t], z); o[t] = z[0]; }
      if ((g & 1) == 0) {
        const int col4 = 16 * (2 * wid + (g >> 1)) + n;
        st16c(ws, OFF_G + (((col4 >> 2) * 108 + 4 * fg + (l & 3)) * 4 + (col4 & 3)) * 16, gran(pack2(o[0], o[1]), pack2(o[2], o[3]), tag));
      }
    }
    STAMP(12);
    STAMP(14);
    FLUSH();
  } else {
    const int t = l - 16, ks = 4 * xc + (t >> 2), hg = t & 3;
    {
      srd_t s = mksrd(ckv, 4096 * 512 * 2);
#pragma unroll
      for (int u = 0; u < 16; ++u) dma16(s, lane * 16, (128 * ks + 16 * wid + u) * 1024, lds0 + L_BIG + (16 * wid + u) * 1040);
    }
    const u32 tag = __builtin_amdgcn_readfirstlane(epoch_v) + 1;
    vm0bar();
    STAMP(1);
    u32x4 fr[32];
    if (wid < 4) {
#pragma unroll
      for (int T = 0; T < 2; ++T)
#pragma unroll
        for (int s = 0; s < 16; ++s) fr[T * 16 + s] = *(LDSP u32x4*)(smem + L_BIG + (32 * wid + 16 * T + n) * 1040 + s * 64 + g * 16);
    } else {
#pragma unroll
      for (int grp = 0; grp < 2; ++grp)
#pragma unroll
        for (int s = 0; s < 4; ++s) {
          u32x2 r[8];
#pragma unroll
          for (int e = 0; e < 8; ++e) r[e] = *(LDSP u32x2*)(smem + L_BIG + (32 * s + 8 * g + e) * 1040 + (128 * (wid - 4) + 64 * grp + 4 * n) * 2);
#pragma unroll
          for (int tt = 0; tt < 4; ++tt) {
            const u32 sel = (tt & 1) ? 0x07060302u : 0x05040100u;
            u32x4 f = {__builtin_amdgcn_perm(r[1][tt >> 1], r[0][tt >> 1], sel), __builtin_amdgcn_perm(r[3][tt >> 1], r[2][tt >> 1], sel),
                       __builtin_amdgcn_perm(r[5][tt >> 1], r[4][tt >> 1], sel), __builtin_amdgcn_perm(r[7][tt >> 1], r[6][tt >> 1], sel)};
            fr[(grp * 4 + s) * 4 + tt] = f;
          }
        }
    }
#pragma unroll
    for (int i = 0; i < 32; ++i) asm volatile("" : "+v"(fr[i]));
    STAMP(2);
    {
      u32 off[2]; u32x4 gb[2];
#pragma unroll
      for (int u = 0; u < 2; ++u) off[u] = OFF_B + ((8 * hg + wid) * 128 + lane + 64 * u) * 16;
      poll<2>(ws, off, tag, gb);
#pragma unroll
      for (int u = 0; u < 2; ++u) *(LDSP u32x2*)(smem + L_QA + wid * 1056 + (lane + 64 * u) * 8) = u32x2{gb[u][0], gb[u][2]};
    }
    STAMP(3);
    bar();
    float sc[8];
    if (wid < 4) {
      f32x4 a0 = {0, 0, 0, 0}, a1 = {0, 0, 0, 0};
#pragma unroll
      for (int s = 0; s < 16; ++s) {
        u32x4 B = *(LDSP u32x4*)(smem + L_QA + (n & 7) * 1056 + (32 * s + 8 * g) * 2);
        a0 = mfma(fr[s], B, a0);
        a1 = mfma(fr[16 + s], B, a1);
      }
      const float kscale = 0.08838834764831845f * 1.4426950408889634f;
      float m = -3.0e38f;
#pragma unroll
      for (int r = 0; r < 4; ++r) { sc[r] = a0[r] * kscale; sc[4 + r] = a1[r] * kscale; m = fmaxf(m, fmaxf(sc[r], sc[4 + r])); }
      m = xmax32(xmax16(m));
      if (lane < 8) *(LDSP float*)(smem + L_MX + (wid * 8 + lane) * 4) = m;
    } else if (t < 10) {
      srd_t s = mksrd(wo, 4096 * 640 * 2);
#pragma unroll
      for (int u = 0; u < 16; ++u) {
        const int blk = (wid - 4) * 16 + u;
        dma16(s, ((lane >> 3) * 640 + (lane & 7) * 8) * 2, ((512 * xc + 8 * blk) * 640 + 64 * t) * 2, lds0 + L_BIG + blk * 1024);
      }
    }
    STAMP(4);
    bar();
    float Mx = 0;
    if (wid < 4) {
      const int hd = n & 7;
      Mx = fmaxf(fmaxf(*(LDSP float*)(smem + L_MX + (0 * 8 + hd) * 4), *(LDSP float*)(smem + L_MX + (1 * 8 + hd) * 4)),
                 fmaxf(*(LDSP float*)(smem + L_MX + (2 * 8 + hd) * 4), *(LDSP float*)(smem + L_MX + (3 * 8 + hd) * 4)));
      u32 pk[4];
      float ls = 0;
#pragma unroll
      for (int r = 0; r < 4; ++r) {
        pk[r] = pack2(__builtin_amdgcn_exp2f(sc[2 * r] - Mx), __builtin_amdgcn_exp2f(sc[2 * r + 1] - Mx));
        ls += bflo(pk[r]) + bfhi(pk[r]);
      }
      if (n < 8) {
        *(LDSP u32x2*)(smem + L_P + n * 288 + (32 * wid + 4 * g) * 2) = u32x2{pk[0], pk[1]};
        *(LDSP u32x2*)(smem + L_P + n * 288 + (32 * wid + 16 + 4 * g) * 2) = u32x2{pk[2], pk[3]};
      }
      ls = xsum32(xsum16(ls));
      if (lane < 8) *(LDSP float*)(smem + L_MX + 128 + (wid * 8 + lane) * 4) = ls;
    }
    bar();
    STAMP(5);
    if (wid == 0) {
      if (lane < 8) {
        float ls = 0;
#pragma unroll
        for (int w = 0; w < 4; ++w) ls += *(LDSP float*)(smem + L_MX + 128 + (w * 8 + lane) * 4);
        st16c(ws, OFF_ML + ((8 * hg + lane) * 32 + ks) * 16, gran(__float_as_uint(Mx), __float_as_uint(ls), tag));
      }
    } else if (wid >= 4) {
      u32x4 pa[4];
#pragma unroll
      for (int s = 0; s < 4; ++s) pa[s] = *(LDSP u32x4*)(smem + L_P + (n & 7) * 288 + (32 * s + 8 * g) * 2);
#pragma unroll
      for (int grp = 0; grp < 2; ++grp) {
        f32x4 acc[4];
#pragma unroll
        for (int tt = 0; tt < 4; ++tt) acc[tt] = f32x4{0, 0, 0, 0};
#pragma unroll
        for (int s = 0; s < 4; ++s)
#pragma unroll
          for (int tt = 0; tt < 4; ++tt) acc[tt] = mfma(pa[s], fr[(grp * 4 + s) * 4 + tt], acc[tt]);
#pragma unroll
        for (int u = 0; u < 2; ++u) {
          const bool hi = g >= 2;
          float o0 = hi ? acc[0][u + 2] : acc[0][u], o1 = hi ? acc[1][u + 2] : acc[1][u];
          float o2 = hi ? acc[2][u + 2] : acc[2][u], o3 = hi ? acc[3][u + 2] : acc[3][u];
          const int hd = 4 * (g & 1) + u + 2 * (g >> 1);
          st16c(ws, OFF_C + ((((8 * hg + hd) * 4 + (wid - 4)) * 32 + ks) * 32 + 16 * grp + n) * 16, gran(pack2(o0, o1), pack2(o2, o3), tag));
        }
      }
    }
    STAMP(6);
    if (t < 10) {
      vm0bar();
      u32x4 wof[16];
      if (wid < 4) {
#pragma unroll
        for (int s = 0; s < 16; ++s) wof[s] = trfrag(smem + L_BIG + (32 * s + 8 * g + (n >> 2)) * 128 + (16 * wid + 4 * (n & 3)) * 2, 128);
#pragma unroll
        for (int s = 0; s < 16; ++s) asm volatile("" : "+v"(wof[s]));
      }
      STAMP(7);
      if (wid < 4) {
        u32 off[2]; u32x4 gd[2];
#pragma unroll
        for (int u = 0; u < 2; ++u) off[u] = OFF_D + (((xc * 4 + wid) * 4 + 2 * (lane >> 5) + u) * 32 + (lane & 31)) * 16;
        poll<2>(ws, off, tag, gd);
        float v0 = bflo(gd[0][0]) + bflo(gd[1][0]), v1 = bfhi(gd[0][0]) + bfhi(gd[1][0]);
        float v2 = bflo(gd[0][2]) + bflo(gd[1][2]), v3 = bfhi(gd[0][2]) + bfhi(gd[1][2]);
        swap32(v0, v2); swap32(v1, v3);
        *(LDSP u32*)(smem + L_CTX + (wid * 128 + 4 * (lane & 31) + 2 * (lane >> 5)) * 2) = pack2(v0 + v2, v1 + v3);
      }
      STAMP(8);
      bar();
      if (wid < 4) {
        f32x4 acc = {0, 0, 0, 0};
#pragma unroll
        for (int s = 0; s < 16; ++s) acc = mfma(*(LDSP u32x4*)(smem + L_CTX + (32 * s + 8 * g) * 2), wof[s], acc);
        u32x4 gr = gather4(acc[0], tag);
        if (lane < 16 && (lane & 3) == 0) st16c(ws, OFF_E + (xc * 160 + 16 * t + 4 * wid + (lane >> 2)) * 16, gr);
      }
      STAMP(9);
    } else if (t < 15 && wid == 0) {
      const int cg = 5 * xc + (t - 10), a = lane >> 2, gq = lane & 3;
      const float xv = bf2f(__builtin_amdgcn_raw_buffer_load_b16(rx, (16 * cg + 4 * gq + g) * 2, 0, 0));
      u32 off1[1]; u32x4 ge[1];
      off1[0] = OFF_E + ((a & 7) * 160 + 4 * cg + gq) * 16;
      poll<1>(ws, off1, tag, ge);
      const float e = a < 8 ? 1.0f : 0.0f;
      const float hv = rbf(xv + rbf(reduce_a(e * bflo(ge[0][0]), e * bfhi(ge[0][0]), e * bflo(ge[0][2]), e * bfhi(ge[0][2]))));
      STAMP(7);
      u32 off[7]; u32x4 gg[7];
#pragma unroll
      for (int u = 0; u < 7; ++u) { int pr = a + 16 * u; if (pr >= 108) pr = 0; off[u] = OFF_G + ((cg * 108 + pr) * 4 + gq) * 16; }
      poll<7>(ws, off, tag, gg);
      STAMP(8);
      float v0 = 0, v1 = 0, v2 = 0, v3 = 0;
#pragma unroll
      for (int u = 0; u < 7; ++u) {
        const float k = (a + 16 * u < 108) ? 1.0f : 0.0f;
        v0 += k * bflo(gg[u][0]); v1 += k * bfhi(gg[u][0]); v2 += k * bflo(gg[u][2]); v3 += k * bfhi(gg[u][2]);
      }
      const float dn = rbf(reduce_a(v0, v1, v2, v3));
      if ((lane & 12) == 0) out[16 * cg + 4 * gq + g] = (u16)pack2(hv + dn, hv + dn);
      if (blockIdx.x == 8 * 26 && lane == 0) __builtin_amdgcn_raw_buffer_store_b32(tag, ws, 0, 0, 16);
      STAMP(9);
    }
    STAMP(14);
    FLUSH();
  }
}

extern "C" void kernel_launch(void* const* d_in, const int* in_sizes, int n_in, void* d_out, int out_size, void* d_ws,
                              size_t ws_size, hipStream_t stream) {
  mega<<<256, 512, 0, stream>>>((const u16*)d_in[0], (const u16*)d_in[1], (const u16*)d_in[2], (const u16*)d_in[3], (const u16*)d_in[4],
                                (const u16*)d_in[5], (const u16*)d_in[7], (const u16*)d_in[8], (const u16*)d_in[9], (u16*)d_out,
                                (unsigned char*)d_ws);
}
```

```cpp
#define PROBE_BLK 208
#define PROBE_W 0
#define PROBE_J 0
#define PROBE_K 0u
#include <hip/hip_runtime.h>

typedef unsigned short u16;
typedef unsigned u32;
typedef __attribute__((ext_vector_type(4))) u32 u32x4;
typedef __attribute__((ext_vector_type(2))) u32 u32x2;
typedef __attribute__((ext_vector_type(4))) float f32x4;
typedef __attribute__((ext_vector_type(2))) float f32x2;
typedef __attribute__((ext_vector_type(8))) __bf16 bf16x8;
typedef __attribute__((ext_vector_type(2))) __bf16 bf16x2;
typedef __attribute__((ext_vector_type(4))) short s16x4;
typedef __amdgpu_buffer_rsrc_t rsrc_t;
#define DI __device__ __forceinline__
#define LDSP __attribute__((address_space(3)))

constexpr int SPIN_MAX = 200000;
constexpr u32 OFF_A = 4096;
constexpr u32 OFF_B = OFF_A + 32 * 4 * 64 * 16;
constexpr u32 OFF_ML = OFF_B + 32 * 128 * 16;
constexpr u32 OFF_D = OFF_ML + 32 * 32 * 16;
constexpr u32 OFF_E = OFF_D + 8 * 16 * 32 * 16;
constexpr u32 OFF_G = OFF_E + 8 * 160 * 16;
constexpr u32 OFF_C = OFF_G + 40 * 108 * 4 * 16;
constexpr u32 WS_USED = OFF_C + 32 * 4 * 32 * 32 * 16;
constexpr u32 OFF_DBG = 8u << 20;

constexpr int L_BIG = 0;
constexpr int L_WUV = 0, L_WG = 32768, L_WU = 32768 + 20480;
constexpr int L_SM = 133120;
constexpr int L_QA = L_SM;
constexpr int L_P = L_QA + 8 * 1056;
constexpr int L_MX = L_P + 8 * 288;
constexpr int L_CTX = L_MX + 256;
constexpr int L_RED = L_SM;
constexpr int L_Q = L_RED + 4096;
constexpr int L_CC = L_Q + 256;
constexpr int L_H = L_CC + 256;
constexpr int L_GU = L_H + 1280;
constexpr int L_TOTAL = L_SM + 14336;

DI float bf2f(u32 b) { return __uint_as_float(b << 16); }
DI float bfhi(u32 w) { return __uint_as_float(w & 0xffff0000u); }
DI float bflo(u32 w) { return __uint_as_float(w << 16); }
DI u32 pack2(float lo, float hi) { f32x2 v = {lo, hi}; return __builtin_bit_cast(u32, __builtin_convertvector(v, bf16x2)); }
DI float rbf(float f) { return bflo(pack2(f, f)); }
DI void bar() { asm volatile("s_waitcnt lgkmcnt(0)\n\ts_barrier" ::: "memory"); }
#define FENCE() do { asm volatile("" ::: "memory"); __builtin_amdgcn_sched_barrier(0); } while (0)
DI void vm0bar() { asm volatile("s_waitcnt vmcnt(0) lgkmcnt(0)\n\ts_barrier" ::: "memory"); }
template <int C> DI float dpp(float v) { return __builtin_bit_cast(float, __builtin_amdgcn_update_dpp(0, __builtin_bit_cast(int, v), C, 0xf, 0xf, true)); }
constexpr int DPP_X1 = 0xB1, DPP_X2 = 0x4E, DPP_ROR4 = 0x124, DPP_ROR8 = 0x128;
DI void swap32(float& a, float& b) { auto r = __builtin_amdgcn_permlane32_swap(__float_as_uint(a), __float_as_uint(b), false, false); const u32 r0 = r[0], r1 = r[1]; a = __uint_as_float(r0); b = __uint_as_float(r1); }
DI void swap16(float& a, float& b) { auto r = __builtin_amdgcn_permlane16_swap(__float_as_uint(a), __float_as_uint(b), false, false); const u32 r0 = r[0], r1 = r[1]; a = __uint_as_float(r0); b = __uint_as_float(r1); }
DI float xsum16(float v) { float a = v, b = v; swap16(a, b); return a + b; }
DI float xsum32(float v) { float a = v, b = v; swap32(a, b); return a + b; }
DI float xmax16(float v) { float a = v, b = v; swap16(a, b); return fmaxf(a, b); }
DI float xmax32(float v) { float a = v, b = v; swap32(a, b); return fmaxf(a, b); }
DI float wave_sum(float v) { v += dpp<DPP_X1>(v); v += dpp<DPP_X2>(v); v += dpp<DPP_ROR4>(v); v += dpp<DPP_ROR8>(v); return xsum32(xsum16(v)); }
DI float reduce_a(float v0, float v1, float v2, float v3) {
  swap32(v0, v2); float X = v0 + v2;
  swap32(v1, v3); float Y = v1 + v3;
  swap16(X, Y); float Z = X + Y;
  Z += dpp<DPP_ROR4>(Z); Z += dpp<DPP_ROR8>(Z);
  return Z;
}
DI f32x4 mfma(u32x4 a, u32x4 b, f32x4 c) { return __builtin_amdgcn_mfma_f32_16x16x32_bf16(__builtin_bit_cast(bf16x8, a), __builtin_bit_cast(bf16x8, b), c, 0, 0, 0); }
DI rsrc_t mkrsrc(const void* p, u32 bytes) { return __builtin_amdgcn_make_buffer_rsrc((void*)p, 0, bytes, 0x00020000); }
DI u32x4 ld16(rsrc_t r, u32 off) { return __builtin_amdgcn_raw_buffer_load_b128(r, off, 0, 0); }
DI u32x2 ld8(rsrc_t r, u32 off) { return __builtin_amdgcn_raw_buffer_load_b64(r, off, 0, 0); }
DI u32x4 ld16c(rsrc_t r, u32 off) { return __builtin_amdgcn_raw_buffer_load_b128(r, off, 0, 16); }
DI void st16c(rsrc_t r, u32 off, u32x4 v) { __builtin_amdgcn_raw_buffer_store_b128(v, r, off, 0, 16); }
typedef __attribute__((ext_vector_type(4))) int srd_t;
DI srd_t mksrd(const void* p, u32 bytes) {
  unsigned long long a = (unsigned long long)p;
  srd_t s = {(int)(u32)a, (int)(u32)(a >> 32), (int)bytes, 0x00020000};
  return s;
}
DI void dma16(srd_t srd, u32 voff, u32 soff, u32 lds) {
  u32 keep;
  asm volatile("s_nop 4\n\ts_mov_b32 %0, m0\n\ts_mov_b32 m0, %1\n\ts_nop 0\n\tbuffer_load_dwordx4 %2, %3, %4 offen lds\n\ts_mov_b32 m0, %0"
               : "=&s"(keep) : "s"(lds), "v"(voff), "s"(srd), "s"(soff) : "memory");
}
DI u32x2 trread(const char* p) { return __builtin_bit_cast(u32x2, __builtin_amdgcn_ds_read_tr16_b64_v4i16((LDSP s16x4*)p)); }
DI u32x4 trfrag(const char* p, int rowstride) {
  u32x2 a = trread(p), b = trread(p + 4 * rowstride);
  u32x4 r = {a[0], a[1], b[0], b[1]};
  return r;
}
DI bool tagok(u32x4 g, u32 tag) { return g[1] == tag && g[3] == ~tag; }
template <int N> DI void poll(rsrc_t ws, const u32 (&off)[N], u32 tag, u32x4 (&g)[N]) {
  int it = SPIN_MAX;
  bool more;
#pragma nounroll
  do {
    u32 bad = 0;
#pragma unroll
    for (int i = 0; i < N; ++i) g[i] = ld16c(ws, off[i]);
#pragma unroll
    for (int i = 0; i < N; ++i) bad |= (g[i][1] ^ tag) | (g[i][3] ^ ~tag);
    more = __any(bad != 0) && --it > 0;
    asm volatile("" ::: "memory");
  } while (more);
}
DI u32x4 gran(u32 p0, u32 p1, u32 tag) { u32x4 g = {p0, tag, p1, ~tag}; return g; }
DI u32x4 gather4(float v, u32 tag) {
  float v1 = dpp<0x55>(v), v2 = dpp<0xAA>(v), v3 = dpp<0xFF>(v), v0 = dpp<0x00>(v);
  return gran(pack2(v0, v1), pack2(v2, v3), tag);
}
DI float ln_std(float var) { return rbf(__builtin_sqrtf(rbf(rbf(var) + 1e-5f))); }

#define STAMP(i) do { st_[i] = (u32)__builtin_amdgcn_s_memrealtime(); } while (0)
#define FLUSH() do { if (blockIdx.x == PROBE_BLK && wid == PROBE_W) { const u32 lim_ = 4000u + PROBE_K * (st_[PROBE_J] - st_[0]); \
    while ((u32)__builtin_amdgcn_s_memrealtime() - st_[0] < lim_) __builtin_amdgcn_s_sleep(2); } } while (0)

__global__ __launch_bounds__(512, 2) void mega(const u16* x, const u16* ckv, const u16* wq,
                                               const u16* wuk, const u16* wuv, const u16* wo,
                                               const u16* wg, const u16* wu, const u16* wd,
                                               u16* out, unsigned char* wsp) {
  __shared__ __attribute__((aligned(16))) char smem[L_TOTAL];
  const int tid = threadIdx.x, lane = tid & 63;
  const int wid = __builtin_amdgcn_readfirstlane(tid >> 6);
  const int xc = blockIdx.x & 7, l = blockIdx.x >> 3;
  const int g = lane >> 4, n = lane & 15;
  const u32 lds0 = (u32)(size_t)smem;
  rsrc_t ws = mkrsrc(wsp, WS_USED);
  rsrc_t rx = mkrsrc(x, 1280);
  u32 st_[16];
  for (int i = 0; i < 16; ++i) st_[i] = 0;
  const u32 cyc0_ = (u32)__builtin_amdgcn_s_memtime();
  STAMP(0);
  const u32 epoch_v = __builtin_amdgcn_raw_buffer_load_b32(ws, 0, 0, 16);

  if (l < 16) {
    const int h = 4 * xc + (l >> 2), j = l & 3;
    rsrc_t rwq = mkrsrc(wq, 640 * 4096 * 2), rwuk = mkrsrc(wuk, 512 * 4096 * 2), rwd = mkrsrc(wd, 1728 * 640 * 2);
    u32x4 xa = ld16(rx, lane * 16);
    u32 xb = __builtin_amdgcn_raw_buffer_load_b32(rx, 1024 + lane * 4, 0, 0);
    u32 xr[5];
#pragma unroll
    for (int s = 0; s < 5; ++s) xr[s] = __builtin_amdgcn_raw_buffer_load_b16(rx, (160 * j + 20 * wid + 4 * s + g) * 2, 0, 0);
    FENCE();
    u32x4 wqf[5], wkf[4];
#pragma unroll
    for (int s = 0; s < 5; ++s) wqf[s] = ld16(rwq, ((160 * j + 20 * wid + 4 * s + g) * 4096 + h * 128 + 8 * n) * 2);
    FENCE();
#pragma unroll
    for (int s = 0; s < 4; ++s) wkf[s] = ld16(rwuk, ((128 * j + 16 * wid + 4 * g + (n >> 2)) * 4096 + h * 128 + 32 * s + 8 * (n & 3)) * 2);
    FENCE();
    const u32 tag = __builtin_amdgcn_readfirstlane(epoch_v) + 1;
    float sum = bflo(xb) + bfhi(xb);
#pragma unroll
    for (int i = 0; i < 4; ++i) sum += bflo(xa[i]) + bfhi(xa[i]);
    const float mean = wave_sum(sum) * (1.0f / 640.0f);
    float sq = (bflo(xb) - mean) * (bflo(xb) - mean) + (bfhi(xb) - mean) * (bfhi(xb) - mean);
#pragma unroll
    for (int i = 0; i < 4; ++i) sq += (bflo(xa[i]) - mean) * (bflo(xa[i]) - mean) + (bfhi(xa[i]) - mean) * (bfhi(xa[i]) - mean);
    const float xstd = ln_std(wave_sum(sq) * (1.0f / 640.0f)), xmean = rbf(mean);
    STAMP(1);
    {
      f32x4 acc = {0, 0, 0, 0};
#pragma unroll
      for (int s = 0; s < 5; ++s) {
        float a = rbf(bf2f(xr[s]) - xmean) / xstd;
        u32 ab = pack2(a, a) & ((n & 1) ? 0xffff0000u : 0x0000ffffu);
        const int r = (n & 7) >> 1;
        u32x4 A = {r == 0 ? ab : 0u, r == 1 ? ab : 0u, r == 2 ? ab : 0u, r == 3 ? ab : 0u};
        acc = mfma(A, wqf[s], acc);
      }
      if (g < 2) *(LDSP f32x4*)(smem + L_RED + wid * 512 + (8 * n + 4 * g) * 4) = acc;
    }
    bar();
    STAMP(2);
    if (wid == 0) {
      f32x2 v = {0, 0};
#pragma unroll
      for (int w = 0; w < 8; ++w) v += *(LDSP f32x2*)(smem + L_RED + w * 512 + lane * 8);
      st16c(ws, OFF_A + ((h * 4 + j) * 64 + lane) * 16, gran(__float_as_uint(v[0]), __float_as_uint(v[1]), tag));
      STAMP(3);
      u32 off[4]; u32x4 gq[4];
#pragma unroll
      for (int i = 0; i < 4; ++i) off[i] = OFF_A + ((h * 4 + i) * 64 + lane) * 16;
      poll<4>(ws, off, tag, gq);
      float q0 = 0, q1 = 0;
#pragma unroll
      for (int i = 0; i < 4; ++i) { q0 += __uint_as_float(gq[i][0]); q1 += __uint_as_float(gq[i][2]); }
      *(LDSP u32*)(smem + L_Q + lane * 4) = pack2(q0, q1);
      STAMP(4);
    }
    bar();
    {
      f32x4 acc = {0, 0, 0, 0};
#pragma unroll
      for (int s = 0; s < 4; ++s) {
        u32x4 A = *(LDSP u32x4*)(smem + L_Q + (32 * s + 8 * (n & 3)) * 2);
        if (g != (n >> 2)) A = u32x4{0, 0, 0, 0};
        acc = mfma(A, wkf[s], acc);
      }
      const int r = n & 3;
      float v01 = (r & 1) ? acc[1] : acc[0], v23 = (r & 1) ? acc[3] : acc[2];
      float v = (r & 2) ? v23 : v01;
      v += dpp<DPP_X1>(v); v += dpp<DPP_X2>(v);
      float v1 = dpp<0x104>(v), v2 = dpp<0x108>(v), v3 = dpp<0x10C>(v);
      if (n == 0) st16c(ws, OFF_B + (h * 128 + 32 * j + 4 * wid + g) * 16, gran(pack2(v, v1), pack2(v2, v3), tag));
    }
    STAMP(5);
    const int fg = xc + 8 * (l >> 2);
    const bool ffn = fg < 27;
    const int f0 = 64 * fg + 16 * (l & 3);
    {
      srd_t s = mksrd(wuv, 512 * 4096 * 2);
#pragma unroll
      for (int u = 0; u < 4; ++u)
        dma16(s, (g * 4096 + n * 8) * 2, ((128 * j + 16 * wid + 4 * u) * 4096 + h * 128) * 2, lds0 + L_WUV + (16 * wid + 4 * u) * 256);
    }
    u32x2 wdr[8];
    if (ffn && wid < 5) {
#pragma unroll
      for (int e = 0; e < 8; ++e) wdr[e] = ld8(rwd, ((f0 + 4 * g + (e & 3)) * 640 + 64 * (2 * wid + (e >> 2)) + 4 * n) * 2);
    }
    vm0bar();
    u32x4 wvf[4];
#pragma unroll
    for (int s = 0; s < 4; ++s) wvf[s] = trfrag(smem + L_WUV + (32 * s + 8 * g + (n >> 2)) * 256 + (16 * wid + 4 * (n & 3)) * 2, 256);
    u32x4 wdf[4];
#pragma unroll
    for (int t = 0; t < 4; ++t) {
      const u32 sel = (t & 1) ? 0x07060302u : 0x05040100u;
      wdf[t][0] = __builtin_amdgcn_perm(wdr[1][t >> 1], wdr[0][t >> 1], sel);
      wdf[t][1] = __builtin_amdgcn_perm(wdr[3][t >> 1], wdr[2][t >> 1], sel);
      wdf[t][2] = __builtin_amdgcn_perm(wdr[5][t >> 1], wdr[4][t >> 1], sel);
      wdf[t][3] = __builtin_amdgcn_perm(wdr[7][t >> 1], wdr[6][t >> 1], sel);
    }
    STAMP(6);
    {
      const int a = lane >> 2, gq = lane & 3;
      u32 off[2]; u32x4 gm[2];
#pragma unroll
      for (int u = 0; u < 2; ++u) off[u] = OFF_ML + (h * 32 + a + 16 * u) * 16;
      poll<2>(ws, off, tag, gm);
      float m0 = __uint_as_float(gm[0][0]), m1 = __uint_as_float(gm[1][0]);
      float M = fmaxf(m0, m1);
      M = fmaxf(M, dpp<DPP_ROR4>(M)); M = fmaxf(M, dpp<DPP_ROR8>(M)); M = xmax32(xmax16(M));
      float s0 = __builtin_amdgcn_exp2f(m0 - M), s1 = __builtin_amdgcn_exp2f(m1 - M);
      float L = s0 * __uint_as_float(gm[0][2]) + s1 * __uint_as_float(gm[1][2]);
      L += dpp<DPP_ROR4>(L); L += dpp<DPP_ROR8>(L); L = xsum32(xsum16(L));
      const float rl = 1.0f / L; s0 *= rl; s1 *= rl;
      STAMP(7);
      u32x4 gc[2];
#pragma unroll
      for (int u = 0; u < 2; ++u) off[u] = OFF_C + (((h * 4 + j) * 32 + a + 16 * u) * 32 + 4 * wid + gq) * 16;
      poll<2>(ws, off, tag, gc);
      STAMP(8);
      float z = reduce_a(s0 * bflo(gc[0][0]) + s1 * bflo(gc[1][0]), s0 * bfhi(gc[0][0]) + s1 * bfhi(gc[1][0]),
                         s0 * bflo(gc[0][2]) + s1 * bflo(gc[1][2]), s0 * bfhi(gc[0][2]) + s1 * bfhi(gc[1][2]));
      if ((lane & 12) == 0) *(LDSP u16*)(smem + L_CC + (16 * wid + 4 * gq + g) * 2) = (u16)pack2(z, z);
    }
    bar();
    {
      f32x4 acc = {0, 0, 0, 0};
#pragma unroll
      for (int s = 0; s < 4; ++s) acc = mfma(*(LDSP u32x4*)(smem + L_CC + (32 * s + 8 * g) * 2), wvf[s], acc);
      u32x4 gr = gather4(acc[0], tag);
      if (lane < 16 && (lane & 3) == 0) st16c(ws, OFF_D + (((xc * 4 + (l >> 2)) * 4 + j) * 32 + 4 * wid + (lane >> 2)) * 16, gr);
    }
    STAMP(9);
    if (!ffn) { FLUSH(); return; }
    {
      srd_t sg = mksrd(wg, 640 * 1728 * 2), su = mksrd(wu, 640 * 1728 * 2);
#pragma unroll
      for (int u = 0; u < 5; ++u) {
        const int blk = wid * 5 + u;
        const u32 voff = ((lane >> 1) * 1728 + (lane & 1) * 8) * 2;
        if (blk < 20) dma16(sg, voff, (blk * 32 * 1728 + f0) * 2, lds0 + L_WG + blk * 1024);
        else dma16(su, voff, ((blk - 20) * 32 * 1728 + f0) * 2, lds0 + L_WU + (blk - 20) * 1024);
      }
    }
    u32x2 xres = ld8(rx, (32 * wid + (lane & 31)) * 8 < 1280 ? (32 * wid + (lane & 31)) * 8 : 0);
    vm0bar();
    u32x4 guf[10];
    const int mat = wid & 1, kh = (wid >> 1) & 1;
    if (wid < 4) {
      f32x4 cs = {0, 0, 0, 0};
      const u32x4 ones = {0x3f803f80u, 0x3f803f80u, 0x3f803f80u, 0x3f803f80u};
#pragma unroll
      for (int s = 0; s < 10; ++s) {
        guf[s] = trfrag(smem + (mat ? L_WU : L_WG) + (32 * (10 * kh + s) + 8 * g + (n >> 2)) * 32 + 8 * (n & 3), 32);
        cs = mfma(guf[s], ones, cs);
      }
      if (n == 0) *(LDSP f32x4*)(smem + L_GU + 256 + (mat * 2 + kh) * 64 + g * 16) = cs;
    }
    STAMP(10);
    if (wid < 5) {
      u32 off[4]; u32x4 ge[4];
#pragma unroll
      for (int u = 0; u < 4; ++u) off[u] = OFF_E + ((4 * (lane >> 5) + u) * 160 + 32 * wid + (lane & 31)) * 16;
      poll<4>(ws, off, tag, ge);
      float v0 = 0, v1 = 0, v2 = 0, v3 = 0;
#pragma unroll
      for (int u = 0; u < 4; ++u) { v0 += bflo(ge[u][0]); v1 += bfhi(ge[u][0]); v2 += bflo(ge[u][2]); v3 += bfhi(ge[u][2]); }
      swap32(v0, v2); swap32(v1, v3);
      float a0 = rbf(v0 + v2), a1 = rbf(v1 + v3);
      u32 xw = lane < 32 ? xres[0] : xres[1];
      *(LDSP u32*)(smem + L_H + (32 * wid + (lane & 31)) * 8 + (lane >> 5) * 4) = pack2(bflo(xw) + a0, bfhi(xw) + a1);
    }
    STAMP(11);
    bar();
    if (wid < 4) {
      f32x4 acc = {0, 0, 0, 0};
#pragma unroll
      for (int s = 0; s < 10; ++s) acc = mfma(guf[s], *(LDSP u32x4*)(smem + L_H + (32 * (10 * kh + s) + 8 * g) * 2), acc);
      if (n == 0) *(LDSP f32x4*)(smem + L_GU + (mat * 2 + kh) * 64 + g * 16) = acc;
    } else if (wid == 4) {
      u32 hv[5];
      float s1 = 0;
#pragma unroll
      for (int u = 0; u < 5; ++u) { hv[u] = *(LDSP u32*)(smem + L_H + (lane + 64 * u) * 4); s1 += bflo(hv[u]) + bfhi(hv[u]); }
      const float mean2 = wave_sum(s1) * (1.0f / 640.0f);
      float s2 = 0;
#pragma unroll
      for (int u = 0; u < 5; ++u) { float d0 = bflo(hv[u]) - mean2, d1 = bfhi(hv[u]) - mean2; s2 += d0 * d0 + d1 * d1; }
      const float sd = ln_std(wave_sum(s2) * (1.0f / 640.0f));
      if (lane == 0) { f32x2 st = {rbf(mean2), 1.0f / sd}; *(LDSP f32x2*)(smem + L_GU + 512) = st; }
    }
    bar();
    if (wid < 5) {
      const f32x2 st = *(LDSP f32x2*)(smem + L_GU + 512);
      f32x4 ga = *(LDSP f32x4*)(smem + L_GU + 0 * 64 + g * 16) + *(LDSP f32x4*)(smem + L_GU + 1 * 64 + g * 16);
      f32x4 ua = *(LDSP f32x4*)(smem + L_GU + 2 * 64 + g * 16) + *(LDSP f32x4*)(smem + L_GU + 3 * 64 + g * 16);
      f32x4 gc = *(LDSP f32x4*)(smem + L_GU + 256 + 0 * 64 + g * 16) + *(LDSP f32x4*)(smem + L_GU + 256 + 1 * 64 + g * 16);
      f32x4 uc = *(LDSP f32x4*)(smem + L_GU + 256 + 2 * 64 + g * 16) + *(LDSP f32x4*)(smem + L_GU + 256 + 3 * 64 + g * 16);
      float gv[4];
#pragma unroll
      for (int r = 0; r < 4; ++r) {
        float gt = rbf((ga[r] - st[0] * gc[r]) * st[1]), up = rbf((ua[r] - st[0] * uc[r]) * st[1]);
        gv[r] = (gt > 0.0f ? gt : 0.0f * gt) * up;
      }
      const u32 p01 = pack2(gv[0], gv[1]), p23 = pack2(gv[2], gv[3]);
      u32x4 A = n < 8 ? u32x4{p01, p23, 0, 0} : u32x4{0, 0, p01, p23};
      float o[4];
#pragma unroll
      for (int t = 0; t < 4; ++t) { f32x4 z = {0, 0, 0, 0}; z = mfma(A, wdf[t], z); o[t] = z[0]; }
      if ((g & 1) == 0) {
        const int col4 = 16 * (2 * wid + (g >> 1)) + n;
        st16c(ws, OFF_G + (((col4 >> 2) * 108 + 4 * fg + (l & 3)) * 4 + (col4 & 3)) * 16, gran(pack2(o[0], o[1]), pack2(o[2], o[3]), tag));
      }
    }
    STAMP(12);
    STAMP(14);
    FLUSH();
  } else {
    const int t = l - 16, ks = 4 * xc + (t >> 2), hg = t & 3;
    {
      srd_t s = mksrd(ckv, 4096 * 512 * 2);
#pragma unroll
      for (int u = 0; u < 16; ++u) dma16(s, lane * 16, (128 * ks + 16 * wid + u) * 1024, lds0 + L_BIG + (16 * wid + u) * 1040);
    }
    const u32 tag = __builtin_amdgcn_readfirstlane(epoch_v) + 1;
    vm0bar();
    STAMP(1);
    u32x4 fr[32];
    if (wid < 4) {
#pragma unroll
      for (int T = 0; T < 2; ++T)
#pragma unroll
        for (int s = 0; s < 16; ++s) fr[T * 16 + s] = *(LDSP u32x4*)(smem + L_BIG + (32 * wid + 16 * T + n) * 1040 + s * 64 + g * 16);
    } else {
#pragma unroll
      for (int grp = 0; grp < 2; ++grp)
#pragma unroll
        for (int s = 0; s < 4; ++s) {
          u32x2 r[8];
#pragma unroll
          for (int e = 0; e < 8; ++e) r[e] = *(LDSP u32x2*)(smem + L_BIG + (32 * s + 8 * g + e) * 1040 + (128 * (wid - 4) + 64 * grp + 4 * n) * 2);
#pragma unroll
          for (int tt = 0; tt < 4; ++tt) {
            const u32 sel = (tt & 1) ? 0x07060302u : 0x05040100u;
            u32x4 f = {__builtin_amdgcn_perm(r[1][tt >> 1], r[0][tt >> 1], sel), __builtin_amdgcn_perm(r[3][tt >> 1], r[2][tt >> 1], sel),
                       __builtin_amdgcn_perm(r[5][tt >> 1], r[4][tt >> 1], sel), __builtin_amdgcn_perm(r[7][tt >> 1], r[6][tt >> 1], sel)};
            fr[(grp * 4 + s) * 4 + tt] = f;
          }
        }
    }
#pragma unroll
    for (int i = 0; i < 32; ++i) asm volatile("" : "+v"(fr[i]));
    STAMP(2);
    {
      u32 off[2]; u32x4 gb[2];
#pragma unroll
      for (int u = 0; u < 2; ++u) off[u] = OFF_B + ((8 * hg + wid) * 128 + lane + 64 * u) * 16;
      poll<2>(ws, off, tag, gb);
#pragma unroll
      for (int u = 0; u < 2; ++u) *(LDSP u32x2*)(smem + L_QA + wid * 1056 + (lane + 64 * u) * 8) = u32x2{gb[u][0], gb[u][2]};
    }
    STAMP(3);
    bar();
    float sc[8];
    if (wid < 4) {
      f32x4 a0 = {0, 0, 0, 0}, a1 = {0, 0, 0, 0};
#pragma unroll
      for (int s = 0; s < 16; ++s) {
        u32x4 B = *(LDSP u32x4*)(smem + L_QA + (n & 7) * 1056 + (32 * s + 8 * g) * 2);
        a0 = mfma(fr[s], B, a0);
        a1 = mfma(fr[16 + s], B, a1);
      }
      const float kscale = 0.08838834764831845f * 1.4426950408889634f;
      float m = -3.0e38f;
#pragma unroll
      for (int r = 0; r < 4; ++r) { sc[r] = a0[r] * kscale; sc[4 + r] = a1[r] * kscale; m = fmaxf(m, fmaxf(sc[r], sc[4 + r])); }
      m = xmax32(xmax16(m));
      if (lane < 8) *(LDSP float*)(smem + L_MX + (wid * 8 + lane) * 4) = m;
    } else if (t < 10) {
      srd_t s = mksrd(wo, 4096 * 640 * 2);
#pragma unroll
      for (int u = 0; u < 16; ++u) {
        const int blk = (wid - 4) * 16 + u;
        dma16(s, ((lane >> 3) * 640 + (lane & 7) * 8) * 2, ((512 * xc + 8 * blk) * 640 + 64 * t) * 2, lds0 + L_BIG + blk * 1024);
      }
    }
    STAMP(4);
    bar();
    float Mx = 0;
    if (wid < 4) {
      const int hd = n & 7;
      Mx = fmaxf(fmaxf(*(LDSP float*)(smem + L_MX + (0 * 8 + hd) * 4), *(LDSP float*)(smem + L_MX + (1 * 8 + hd) * 4)),
                 fmaxf(*(LDSP float*)(smem + L_MX + (2 * 8 + hd) * 4), *(LDSP float*)(smem + L_MX + (3 * 8 + hd) * 4)));
      u32 pk[4];
      float ls = 0;
#pragma unroll
      for (int r = 0; r < 4; ++r) {
        pk[r] = pack2(__builtin_amdgcn_exp2f(sc[2 * r] - Mx), __builtin_amdgcn_exp2f(sc[2 * r + 1] - Mx));
        ls += bflo(pk[r]) + bfhi(pk[r]);
      }
      if (n < 8) {
        *(LDSP u32x2*)(smem + L_P + n * 288 + (32 * wid + 4 * g) * 2) = u32x2{pk[0], pk[1]};
        *(LDSP u32x2*)(smem + L_P + n * 288 + (32 * wid + 16 + 4 * g) * 2) = u32x2{pk[2], pk[3]};
      }
      ls = xsum32(xsum16(ls));
      if (lane < 8) *(LDSP float*)(smem + L_MX + 128 + (wid * 8 + lane) * 4) = ls;
    }
    bar();
    STAMP(5);
    if (wid == 0) {
      if (lane < 8) {
        float ls = 0;
#pragma unroll
        for (int w = 0; w < 4; ++w) ls += *(LDSP float*)(smem + L_MX + 128 + (w * 8 + lane) * 4);
        st16c(ws, OFF_ML + ((8 * hg + lane) * 32 + ks) * 16, gran(__float_as_uint(Mx), __float_as_uint(ls), tag));
      }
    } else if (wid >= 4) {
      u32x4 pa[4];
#pragma unroll
      for (int s = 0; s < 4; ++s) pa[s] = *(LDSP u32x4*)(smem + L_P + (n & 7) * 288 + (32 * s + 8 * g) * 2);
#pragma unroll
      for (int grp = 0; grp < 2; ++grp) {
        f32x4 acc[4];
#pragma unroll
        for (int tt = 0; tt < 4; ++tt) acc[tt] = f32x4{0, 0, 0, 0};
#pragma unroll
        for (int s = 0; s < 4; ++s)
#pragma unroll
          for (int tt = 0; tt < 4; ++tt) acc[tt] = mfma(pa[s], fr[(grp * 4 + s) * 4 + tt], acc[tt]);
#pragma unroll
        for (int u = 0; u < 2; ++u) {
          const bool hi = g >= 2;
          float o0 = hi ? acc[0][u + 2] : acc[0][u], o1 = hi ? acc[1][u + 2] : acc[1][u];
          float o2 = hi ? acc[2][u + 2] : acc[2][u], o3 = hi ? acc[3][u + 2] : acc[3][u];
          const int hd = 4 * (g & 1) + u + 2 * (g >> 1);
          st16c(ws, OFF_C + ((((8 * hg + hd) * 4 + (wid - 4)) * 32 + ks) * 32 + 16 * grp + n) * 16, gran(pack2(o0, o1), pack2(o2, o3), tag));
        }
      }
    }
    STAMP(6);
    if (t < 10) {
      vm0bar();
      u32x4 wof[16];
      if (wid < 4) {
#pragma unroll
        for (int s = 0; s < 16; ++s) wof[s] = trfrag(smem + L_BIG + (32 * s + 8 * g + (n >> 2)) * 128 + (16 * wid + 4 * (n & 3)) * 2, 128);
#pragma unroll
        for (int s = 0; s < 16; ++s) asm volatile("" : "+v"(wof[s]));
      }
      STAMP(7);
      if (wid < 4) {
        u32 off[2]; u32x4 gd[2];
#pragma unroll
        for (int u = 0; u < 2; ++u) off[u] = OFF_D + (((xc * 4 + wid) * 4 + 2 * (lane >> 5) + u) * 32 + (lane & 31)) * 16;
        poll<2>(ws, off, tag, gd);
        float v0 = bflo(gd[0][0]) + bflo(gd[1][0]), v1 = bfhi(gd[0][0]) + bfhi(gd[1][0]);
        float v2 = bflo(gd[0][2]) + bflo(gd[1][2]), v3 = bfhi(gd[0][2]) + bfhi(gd[1][2]);
        swap32(v0, v2); swap32(v1, v3);
        *(LDSP u32*)(smem + L_CTX + (wid * 128 + 4 * (lane & 31) + 2 * (lane >> 5)) * 2) = pack2(v0 + v2, v1 + v3);
      }
      STAMP(8);
      bar();
      if (wid < 4) {
        f32x4 acc = {0, 0, 0, 0};
#pragma unroll
        for (int s = 0; s < 16; ++s) acc = mfma(*(LDSP u32x4*)(smem + L_CTX + (32 * s + 8 * g) * 2), wof[s], acc);
        u32x4 gr = gather4(acc[0], tag);
        if (lane < 16 && (lane & 3) == 0) st16c(ws, OFF_E + (xc * 160 + 16 * t + 4 * wid + (lane >> 2)) * 16, gr);
      }
      STAMP(9);
    } else if (t < 15 && wid == 0) {
      const int cg = 5 * xc + (t - 10), a = lane >> 2, gq = lane & 3;
      const float xv = bf2f(__builtin_amdgcn_raw_buffer_load_b16(rx, (16 * cg + 4 * gq + g) * 2, 0, 0));
      u32 off1[1]; u32x4 ge[1];
      off1[0] = OFF_E + ((a & 7) * 160 + 4 * cg + gq) * 16;
      poll<1>(ws, off1, tag, ge);
      const float e = a < 8 ? 1.0f : 0.0f;
      const float hv = rbf(xv + rbf(reduce_a(e * bflo(ge[0][0]), e * bfhi(ge[0][0]), e * bflo(ge[0][2]), e * bfhi(ge[0][2]))));
      STAMP(7);
      u32 off[7]; u32x4 gg[7];
#pragma unroll
      for (int u = 0; u < 7; ++u) { int pr = a + 16 * u; if (pr >= 108) pr = 0; off[u] = OFF_G + ((cg * 108 + pr) * 4 + gq) * 16; }
      poll<7>(ws, off, tag, gg);
      STAMP(8);
      float v0 = 0, v1 = 0, v2 = 0, v3 = 0;
#pragma unroll
      for (int u = 0; u < 7; ++u) {
        const float k = (a + 16 * u < 108) ? 1.0f : 0.0f;
        v0 += k * bflo(gg[u][0]); v1 += k * bfhi(gg[u][0]); v2 += k * bflo(gg[u][2]); v3 += k * bfhi(gg[u][2]);
      }
      const float dn = rbf(reduce_a(v0, v1, v2, v3));
      if ((lane & 12) == 0) out[16 * cg + 4 * gq + g] = (u16)pack2(hv + dn, hv + dn);
      if (blockIdx.x == 8 * 26 && lane == 0) __builtin_amdgcn_raw_buffer_store_b32(tag, ws, 0, 0, 16);
      STAMP(9);
    }
    STAMP(14);
    FLUSH();
  }
}

extern "C" void kernel_launch(void* const* d_in, const int* in_sizes, int n_in, void* d_out, int out_size, void* d_ws,
                              size_t ws_size, hipStream_t stream) {
  mega<<<256, 512, 0, stream>>>((const u16*)d_in[0], (const u16*)d_in[1], (const u16*)d_in[2], (const u16*)d_in[3], (const u16*)d_in[4],
                                (const u16*)d_in[5], (const u16*)d_in[7], (const u16*)d_in[8], (const u16*)d_in[9], (u16*)d_out,
                                (unsigned char*)d_ws);
}
```

```cpp
#define PROBE_MODE 3
#define PB 208
#define PJ 0
#include <hip/hip_runtime.h>

typedef unsigned short u16;
typedef unsigned u32;
typedef __attribute__((ext_vector_type(4))) u32 u32x4;
typedef __attribute__((ext_vector_type(2))) u32 u32x2;
typedef __attribute__((ext_vector_type(4))) float f32x4;
typedef __attribute__((ext_vector_type(2))) float f32x2;
typedef __attribute__((ext_vector_type(8))) __bf16 bf16x8;
typedef __attribute__((ext_vector_type(2))) __bf16 bf16x2;
typedef __attribute__((ext_vector_type(4))) short s16x4;
typedef __amdgpu_buffer_rsrc_t rsrc_t;
#define DI __device__ __forceinline__
#define LDSP __attribute__((address_space(3)))

constexpr int SPIN_MAX = 200000;
constexpr u32 OFF_A = 4096;
constexpr u32 OFF_B = OFF_A + 32 * 4 * 64 * 16;
constexpr u32 OFF_ML = OFF_B + 32 * 128 * 16;
constexpr u32 OFF_D = OFF_ML + 32 * 32 * 16;
constexpr u32 OFF_E = OFF_D + 8 * 16 * 32 * 16;
constexpr u32 OFF_G = OFF_E + 8 * 160 * 16;
constexpr u32 OFF_C = OFF_G + 40 * 108 * 4 * 16;
constexpr u32 WS_USED = OFF_C + 32 * 4 * 32 * 32 * 16;
constexpr u32 OFF_DBG = 8u << 20;

constexpr int L_BIG = 0;
constexpr int L_WUV = 0, L_WG = 32768, L_WU = 32768 + 20480;
constexpr int L_SM = 133120;
constexpr int L_QA = L_SM;
constexpr int L_P = L_QA + 8 * 1056;
constexpr int L_MX = L_P + 8 * 288;
constexpr int L_CTX = L_MX + 256;
constexpr int L_RED = L_SM;
constexpr int L_Q = L_RED + 4096;
constexpr int L_CC = L_Q + 256;
constexpr int L_H = L_CC + 256;
constexpr int L_GU = L_H + 1280;
constexpr int L_TOTAL = L_SM + 14336;

DI float bf2f(u32 b) { return __uint_as_float(b << 16); }
DI float bfhi(u32 w) { return __uint_as_float(w & 0xffff0000u); }
DI float bflo(u32 w) { return __uint_as_float(w << 16); }
DI u32 pack2(float lo, float hi) { f32x2 v = {lo, hi}; return __builtin_bit_cast(u32, __builtin_convertvector(v, bf16x2)); }
DI float rbf(float f) { return bflo(pack2(f, f)); }
DI void bar() { asm volatile("s_waitcnt lgkmcnt(0)\n\ts_barrier" ::: "memory"); }
#define FENCE() do { asm volatile("" ::: "memory"); __builtin_amdgcn_sched_barrier(0); } while (0)
DI void vm0bar() { asm volatile("s_waitcnt vmcnt(0) lgkmcnt(0)\n\ts_barrier" ::: "memory"); }
template <int C> DI float dpp(float v) { return __builtin_bit_cast(float, __builtin_amdgcn_update_dpp(0, __builtin_bit_cast(int, v), C, 0xf, 0xf, true)); }
constexpr int DPP_X1 = 0xB1, DPP_X2 = 0x4E, DPP_ROR4 = 0x124, DPP_ROR8 = 0x128;
DI void swap32(float& a, float& b) { auto r = __builtin_amdgcn_permlane32_swap(__float_as_uint(a), __float_as_uint(b), false, false); const u32 r0 = r[0], r1 = r[1]; a = __uint_as_float(r0); b = __uint_as_float(r1); }
DI void swap16(float& a, float& b) { auto r = __builtin_amdgcn_permlane16_swap(__float_as_uint(a), __float_as_uint(b), false, false); const u32 r0 = r[0], r1 = r[1]; a = __uint_as_float(r0); b = __uint_as_float(r1); }
DI float xsum16(float v) { float a = v, b = v; swap16(a, b); return a + b; }
DI float xsum32(float v) { float a = v, b = v; swap32(a, b); return a + b; }
DI float xmax16(float v) { float a = v, b = v; swap16(a, b); return fmaxf(a, b); }
DI float xmax32(float v) { float a = v, b = v; swap32(a, b); return fmaxf(a, b); }
DI float wave_sum(float v) { v += dpp<DPP_X1>(v); v += dpp<DPP_X2>(v); v += dpp<DPP_ROR4>(v); v += dpp<DPP_ROR8>(v); return xsum32(xsum16(v)); }
DI float reduce_a(float v0, float v1, float v2, float v3) {
  swap32(v0, v2); float X = v0 + v2;
  swap32(v1, v3); float Y = v1 + v3;
  swap16(X, Y); float Z = X + Y;
  Z += dpp<DPP_ROR4>(Z); Z += dpp<DPP_ROR8>(Z);
  return Z;
}
DI f32x4 mfma(u32x4 a, u32x4 b, f32x4 c) { return __builtin_amdgcn_mfma_f32_16x16x32_bf16(__builtin_bit_cast(bf16x8, a), __builtin_bit_cast(bf16x8, b), c, 0, 0, 0); }
DI rsrc_t mkrsrc(const void* p, u32 bytes) { return __builtin_amdgcn_make_buffer_rsrc((void*)p, 0, bytes, 0x00020000); }
DI u32x4 ld16(rsrc_t r, u32 off) { return __builtin_amdgcn_raw_buffer_load_b128(r, off, 0, 0); }
DI u32x2 ld8(rsrc_t r, u32 off) { return __builtin_amdgcn_raw_buffer_load_b64(r, off, 0, 0); }
DI u32x4 ld16c(rsrc_t r, u32 off) { return __builtin_amdgcn_raw_buffer_load_b128(r, off, 0, 16); }
DI void st16c(rsrc_t r, u32 off, u32x4 v) { __builtin_amdgcn_raw_buffer_store_b128(v, r, off, 0, 16); }
typedef __attribute__((ext_vector_type(4))) int srd_t;
DI srd_t mksrd(const void* p, u32 bytes) {
  unsigned long long a = (unsigned long long)p;
  srd_t s = {(int)(u32)a, (int)(u32)(a >> 32), (int)bytes, 0x00020000};
  return s;
}
DI void dma16(srd_t srd, u32 voff, u32 soff, u32 lds) {
  u32 keep;
  asm volatile("s_nop 4\n\ts_mov_b32 %0, m0\n\ts_mov_b32 m0, %1\n\ts_nop 0\n\tbuffer_load_dwordx4 %2, %3, %4 offen lds\n\ts_mov_b32 m0, %0"
               : "=&s"(keep) : "s"(lds), "v"(voff), "s"(srd), "s"(soff) : "memory");
}
DI u32x2 trread(const char* p) { return __builtin_bit_cast(u32x2, __builtin_amdgcn_ds_read_tr16_b64_v4i16((LDSP s16x4*)p)); }
DI u32x4 trfrag(const char* p, int rowstride) {
  u32x2 a = trread(p), b = trread(p + 4 * rowstride);
  u32x4 r = {a[0], a[1], b[0], b[1]};
  return r;
}
DI bool tagok(u32x4 g, u32 tag) { return g[1] == tag && g[3] == ~tag; }
template <int N> DI void poll(rsrc_t ws, const u32 (&off)[N], u32 tag, u32x4 (&g)[N]) {
  int it = SPIN_MAX;
  bool more;
#pragma nounroll
  do {
    u32 bad = 0;
#pragma unroll
    for (int i = 0; i < N; ++i) g[i] = ld16c(ws, off[i]);
#pragma unroll
    for (int i = 0; i < N; ++i) bad |= (g[i][1] ^ tag) | (g[i][3] ^ ~tag);
    more = __any(bad != 0) && --it > 0;
    asm volatile("" ::: "memory");
  } while (more);
}
DI u32x4 gran(u32 p0, u32 p1, u32 tag) { u32x4 g = {p0, tag, p1, ~tag}; return g; }
DI u32x4 gather4(float v, u32 tag) {
  float v1 = dpp<0x55>(v), v2 = dpp<0xAA>(v), v3 = dpp<0xFF>(v), v0 = dpp<0x00>(v);
  return gran(pack2(v0, v1), pack2(v2, v3), tag);
}
DI float ln_std(float var) { return rbf(__builtin_sqrtf(rbf(rbf(var) + 1e-5f))); }

#define STAMP(i) do { st_[i] = (u32)__builtin_amdgcn_s_memrealtime(); } while (0)
#define FLUSH()

__global__ __launch_bounds__(512, 2) void mega(const u16* x, const u16* ckv, const u16* wq,
                                               const u16* wuk, const u16* wuv, const u16* wo,
                                               const u16* wg, const u16* wu, const u16* wd,
                                               u16* out, unsigned char* wsp) {
  __shared__ __attribute__((aligned(16))) char smem[L_TOTAL];
  const int tid = threadIdx.x, lane = tid & 63;
  const int wid = __builtin_amdgcn_readfirstlane(tid >> 6);
  const int xc = blockIdx.x & 7, l = blockIdx.x >> 3;
  const int g = lane >> 4, n = lane & 15;
  const u32 lds0 = (u32)(size_t)smem;
  rsrc_t ws = mkrsrc(wsp, WS_USED);
  rsrc_t rx = mkrsrc(x, 1280);
  u32 st_[16];
  for (int i = 0; i < 16; ++i) st_[i] = 0;
  const u32 cyc0_ = (u32)__builtin_amdgcn_s_memtime();
  STAMP(0);
  const u32 epoch_v = __builtin_amdgcn_raw_buffer_load_b32(ws, 0, 0, 16);

  if (l < 16) {
    const int h = 4 * xc + (l >> 2), j = l & 3;
    rsrc_t rwq = mkrsrc(wq, 640 * 4096 * 2), rwuk = mkrsrc(wuk, 512 * 4096 * 2), rwd = mkrsrc(wd, 1728 * 640 * 2);
    u32x4 xa = ld16(rx, lane * 16);
    u32 xb = __builtin_amdgcn_raw_buffer_load_b32(rx, 1024 + lane * 4, 0, 0);
    u32 xr[5];
#pragma unroll
    for (int s = 0; s < 5; ++s) xr[s] = __builtin_amdgcn_raw_buffer_load_b16(rx, (160 * j + 20 * wid + 4 * s + g) * 2, 0, 0);
    FENCE();
    u32x4 wqf[5], wkf[4];
#pragma unroll
    for (int s = 0; s < 5; ++s) wqf[s] = ld16(rwq, ((160 * j + 20 * wid + 4 * s + g) * 4096 + h * 128 + 8 * n) * 2);
    FENCE();
#pragma unroll
    for (int s = 0; s < 4; ++s) wkf[s] = ld16(rwuk, ((128 * j + 16 * wid + 4 * g + (n >> 2)) * 4096 + h * 128 + 32 * s + 8 * (n & 3)) * 2);
    FENCE();
    const u32 tag = __builtin_amdgcn_readfirstlane(epoch_v) + 1;
    float sum = bflo(xb) + bfhi(xb);
#pragma unroll
    for (int i = 0; i < 4; ++i) sum += bflo(xa[i]) + bfhi(xa[i]);
    const float mean = wave_sum(sum) * (1.0f / 640.0f);
    float sq = (bflo(xb) - mean) * (bflo(xb) - mean) + (bfhi(xb) - mean) * (bfhi(xb) - mean);
#pragma unroll
    for (int i = 0; i < 4; ++i) sq += (bflo(xa[i]) - mean) * (bflo(xa[i]) - mean) + (bfhi(xa[i]) - mean) * (bfhi(xa[i]) - mean);
    const float xstd = ln_std(wave_sum(sq) * (1.0f / 640.0f)), xmean = rbf(mean);
    STAMP(1);
    {
      f32x4 acc = {0, 0, 0, 0};
#pragma unroll
      for (int s = 0; s < 5; ++s) {
        float a = rbf(bf2f(xr[s]) - xmean) / xstd;
        u32 ab = pack2(a, a) & ((n & 1) ? 0xffff0000u : 0x0000ffffu);
        const int r = (n & 7) >> 1;
        u32x4 A = {r == 0 ? ab : 0u, r == 1 ? ab : 0u, r == 2 ? ab : 0u, r == 3 ? ab : 0u};
        acc = mfma(A, wqf[s], acc);
      }
      if (g < 2) *(LDSP f32x4*)(smem + L_RED + wid * 512 + (8 * n + 4 * g) * 4) = acc;
    }
    bar();
    STAMP(2);
    if (wid == 0) {
      f32x2 v = {0, 0};
#pragma unroll
      for (int w = 0; w < 8; ++w) v += *(LDSP f32x2*)(smem + L_RED + w * 512 + lane * 8);
      st16c(ws, OFF_A + ((h * 4 + j) * 64 + lane) * 16, gran(__float_as_uint(v[0]), __float_as_uint(v[1]), tag));
      STAMP(3);
      u32 off[4]; u32x4 gq[4];
#pragma unroll
      for (int i = 0; i < 4; ++i) off[i] = OFF_A + ((h * 4 + i) * 64 + lane) * 16;
      poll<4>(ws, off, tag, gq);
      float q0 = 0, q1 = 0;
#pragma unroll
      for (int i = 0; i < 4; ++i) { q0 += __uint_as_float(gq[i][0]); q1 += __uint_as_float(gq[i][2]); }
      *(LDSP u32*)(smem + L_Q + lane * 4) = pack2(q0, q1);
      STAMP(4);
    }
    bar();
    {
      f32x4 acc = {0, 0, 0, 0};
#pragma unroll
      for (int s = 0; s < 4; ++s) {
        u32x4 A = *(LDSP u32x4*)(smem + L_Q + (32 * s + 8 * (n & 3)) * 2);
        if (g != (n >> 2)) A = u32x4{0, 0, 0, 0};
        acc = mfma(A, wkf[s], acc);
      }
      const int r = n & 3;
      float v01 = (r & 1) ? acc[1] : acc[0], v23 = (r & 1) ? acc[3] : acc[2];
      float v = (r & 2) ? v23 : v01;
      v += dpp<DPP_X1>(v); v += dpp<DPP_X2>(v);
      float v1 = dpp<0x104>(v), v2 = dpp<0x108>(v), v3 = dpp<0x10C>(v);
      if (n == 0) st16c(ws, OFF_B + (h * 128 + 32 * j + 4 * wid + g) * 16, gran(pack2(v, v1), pack2(v2, v3), tag));
    }
    STAMP(5);
    const int fg = xc + 8 * (l >> 2);
    const bool ffn = fg < 27;
    const int f0 = 64 * fg + 16 * (l & 3);
    {
      srd_t s = mksrd(wuv, 512 * 4096 * 2);
#pragma unroll
      for (int u = 0; u < 4; ++u)
        dma16(s, (g * 4096 + n * 8) * 2, ((128 * j + 16 * wid + 4 * u) * 4096 + h * 128) * 2, lds0 + L_WUV + (16 * wid + 4 * u) * 256);
    }
    u32x2 wdr[8];
    if (ffn && wid < 5) {
#pragma unroll
      for (int e = 0; e < 8; ++e) wdr[e] = ld8(rwd, ((f0 + 4 * g + (e & 3)) * 640 + 64 * (2 * wid + (e >> 2)) + 4 * n) * 2);
    }
    vm0bar();
    u32x4 wvf[4];
#pragma unroll
    for (int s = 0; s < 4; ++s) wvf[s] = trfrag(smem + L_WUV + (32 * s + 8 * g + (n >> 2)) * 256 + (16 * wid + 4 * (n & 3)) * 2, 256);
    u32x4 wdf[4];
#pragma unroll
    for (int t = 0; t < 4; ++t) {
      const u32 sel = (t & 1) ? 0x07060302u : 0x05040100u;
      wdf[t][0] = __builtin_amdgcn_perm(wdr[1][t >> 1], wdr[0][t >> 1], sel);
      wdf[t][1] = __builtin_amdgcn_perm(wdr[3][t >> 1], wdr[2][t >> 1], sel);
      wdf[t][2] = __builtin_amdgcn_perm(wdr[5][t >> 1], wdr[4][t >> 1], sel);
      wdf[t][3] = __builtin_amdgcn_perm(wdr[7][t >> 1], wdr[6][t >> 1], sel);
    }
    STAMP(6);
    {
      const int a = lane >> 2, gq = lane & 3;
      u32 off[2]; u32x4 gm[2];
#pragma unroll
      for (int u = 0; u < 2; ++u) off[u] = OFF_ML + (h * 32 + a + 16 * u) * 16;
      poll<2>(ws, off, tag, gm);
      float m0 = __uint_as_float(gm[0][0]), m1 = __uint_as_float(gm[1][0]);
      float M = fmaxf(m0, m1);
      M = fmaxf(M, dpp<DPP_ROR4>(M)); M = fmaxf(M, dpp<DPP_ROR8>(M)); M = xmax32(xmax16(M));
      float s0 = __builtin_amdgcn_exp2f(m0 - M), s1 = __builtin_amdgcn_exp2f(m1 - M);
      float L = s0 * __uint_as_float(gm[0][2]) + s1 * __uint_as_float(gm[1][2]);
      L += dpp<DPP_ROR4>(L); L += dpp<DPP_ROR8>(L); L = xsum32(xsum16(L));
      const float rl = 1.0f / L; s0 *= rl; s1 *= rl;
      STAMP(7);
      u32x4 gc[2];
#pragma unroll
      for (int u = 0; u < 2; ++u) off[u] = OFF_C + (((h * 4 + j) * 32 + a + 16 * u) * 32 + 4 * wid + gq) * 16;
      poll<2>(ws, off, tag, gc);
      STAMP(8);
      float z = reduce_a(s0 * bflo(gc[0][0]) + s1 * bflo(gc[1][0]), s0 * bfhi(gc[0][0]) + s1 * bfhi(gc[1][0]),
                         s0 * bflo(gc[0][2]) + s1 * bflo(gc[1][2]), s0 * bfhi(gc[0][2]) + s1 * bfhi(gc[1][2]));
      if ((lane & 12) == 0) *(LDSP u16*)(smem + L_CC + (16 * wid + 4 * gq + g) * 2) = (u16)pack2(z, z);
    }
    bar();
    {
      f32x4 acc = {0, 0, 0, 0};
#pragma unroll
      for (int s = 0; s < 4; ++s) acc = mfma(*(LDSP u32x4*)(smem + L_CC + (32 * s + 8 * g) * 2), wvf[s], acc);
      u32x4 gr = gather4(acc[0], tag);
      if (lane < 16 && (lane & 3) == 0) st16c(ws, OFF_D + (((xc * 4 + (l >> 2)) * 4 + j) * 32 + 4 * wid + (lane >> 2)) * 16, gr);
    }
    STAMP(9);
    if (!ffn) { FLUSH(); return; }
    {
      srd_t sg = mksrd(wg, 640 * 1728 * 2), su = mksrd(wu, 640 * 1728 * 2);
#pragma unroll
      for (int u = 0; u < 5; ++u) {
        const int blk = wid * 5 + u;
        const u32 voff = ((lane >> 1) * 1728 + (lane & 1) * 8) * 2;
        if (blk < 20) dma16(sg, voff, (blk * 32 * 1728 + f0) * 2, lds0 + L_WG + blk * 1024);
        else dma16(su, voff, ((blk - 20) * 32 * 1728 + f0) * 2, lds0 + L_WU + (blk - 20) * 1024);
      }
    }
    u32x2 xres = ld8(rx, (32 * wid + (lane & 31)) * 8 < 1280 ? (32 * wid + (lane & 31)) * 8 : 0);
    vm0bar();
    u32x4 guf[10];
    const int mat = wid & 1, kh = (wid >> 1) & 1;
    if (wid < 4) {
      f32x4 cs = {0, 0, 0, 0};
      const u32x4 ones = {0x3f803f80u, 0x3f803f80u, 0x3f803f80u, 0x3f803f80u};
#pragma unroll
      for (int s = 0; s < 10; ++s) {
        guf[s] = trfrag(smem + (mat ? L_WU : L_WG) + (32 * (10 * kh + s) + 8 * g + (n >> 2)) * 32 + 8 * (n & 3), 32);
        cs = mfma(guf[s], ones, cs);
      }
      if (n == 0) *(LDSP f32x4*)(smem + L_GU + 256 + (mat * 2 + kh) * 64 + g * 16) = cs;
    }
    STAMP(10);
    if (wid < 5) {
      u32 off[4]; u32x4 ge[4];
#pragma unroll
      for (int u = 0; u < 4; ++u) off[u] = OFF_E + ((4 * (lane >> 5) + u) * 160 + 32 * wid + (lane & 31)) * 16;
      poll<4>(ws, off, tag, ge);
      float v0 = 0, v1 = 0, v2 = 0, v3 = 0;
#pragma unroll
      for (int u = 0; u < 4; ++u) { v0 += bflo(ge[u][0]); v1 += bfhi(ge[u][0]); v2 += bflo(ge[u][2]); v3 += bfhi(ge[u][2]); }
      swap32(v0, v2); swap32(v1, v3);
      float a0 = rbf(v0 + v2), a1 = rbf(v1 + v3);
      u32 xw = lane < 32 ? xres[0] : xres[1];
      *(LDSP u32*)(smem + L_H + (32 * wid + (lane & 31)) * 8 + (lane >> 5) * 4) = pack2(bflo(xw) + a0, bfhi(xw) + a1);
    }
    STAMP(11);
    bar();
    if (wid < 4) {
      f32x4 acc = {0, 0, 0, 0};
#pragma unroll
      for (int s = 0; s < 10; ++s) acc = mfma(guf[s], *(LDSP u32x4*)(smem + L_H + (32 * (10 * kh + s) + 8 * g) * 2), acc);
      if (n == 0) *(LDSP f32x4*)(smem + L_GU + (mat * 2 + kh) * 64 + g * 16) = acc;
    } else if (wid == 4) {
      u32 hv[5];
      float s1 = 0;
#pragma unroll
      for (int u = 0; u < 5; ++u) { hv[u] = *(LDSP u32*)(smem + L_H + (lane + 64 * u) * 4); s1 += bflo(hv[u]) + bfhi(hv[u]); }
      const float mean2 = wave_sum(s1) * (1.0f / 640.0f);
      float s2 = 0;
#pragma unroll
      for (int u = 0; u < 5; ++u) { float d0 = bflo(hv[u]) - mean2, d1 = bfhi(hv[u]) - mean2; s2 += d0 * d0 + d1 * d1; }
      const float sd = ln_std(wave_sum(s2) * (1.0f / 640.0f));
      if (lane == 0) { f32x2 st = {rbf(mean2), 1.0f / sd}; *(LDSP f32x2*)(smem + L_GU + 512) = st; }
    }
    bar();
    if (wid < 5) {
      const f32x2 st = *(LDSP f32x2*)(smem + L_GU + 512);
      f32x4 ga = *(LDSP f32x4*)(smem + L_GU + 0 * 64 + g * 16) + *(LDSP f32x4*)(smem + L_GU + 1 * 64 + g * 16);
      f32x4 ua = *(LDSP f32x4*)(smem + L_GU + 2 * 64 + g * 16) + *(LDSP f32x4*)(smem + L_GU + 3 * 64 + g * 16);
      f32x4 gc = *(LDSP f32x4*)(smem + L_GU + 256 + 0 * 64 + g * 16) + *(LDSP f32x4*)(smem + L_GU + 256 + 1 * 64 + g * 16);
      f32x4 uc = *(LDSP f32x4*)(smem + L_GU + 256 + 2 * 64 + g * 16) + *(LDSP f32x4*)(smem + L_GU + 256 + 3 * 64 + g * 16);
      float gv[4];
#pragma unroll
      for (int r = 0; r < 4; ++r) {
        float gt = rbf((ga[r] - st[0] * gc[r]) * st[1]), up = rbf((ua[r] - st[0] * uc[r]) * st[1]);
        gv[r] = (gt > 0.0f ? gt : 0.0f * gt) * up;
      }
      const u32 p01 = pack2(gv[0], gv[1]), p23 = pack2(gv[2], gv[3]);
      u32x4 A = n < 8 ? u32x4{p01, p23, 0, 0} : u32x4{0, 0, p01, p23};
      float o[4];
#pragma unroll
      for (int t = 0; t < 4; ++t) { f32x4 z = {0, 0, 0, 0}; z = mfma(A, wdf[t], z); o[t] = z[0]; }
      if ((g & 1) == 0) {
        const int col4 = 16 * (2 * wid + (g >> 1)) + n;
        st16c(ws, OFF_G + (((col4 >> 2) * 108 + 4 * fg + (l & 3)) * 4 + (col4 & 3)) * 16, gran(pack2(o[0], o[1]), pack2(o[2], o[3]), tag));
      }
    }
    STAMP(12);
    STAMP(14);
    FLUSH();
  } else {
    const int t = l - 16, ks = 4 * xc + (t >> 2), hg = t & 3;
    {
      srd_t s = mksrd(ckv, 4096 * 512 * 2);
#pragma unroll
      for (int u = 0; u < 16; ++u) dma16(s, lane * 16, (128 * ks + 16 * wid + u) * 1024, lds0 + L_BIG + (16 * wid + u) * 1040);
    }
    const u32 tag = __builtin_amdgcn_readfirstlane(epoch_v) + 1;
    vm0bar();
    STAMP(1);
    u32x4 fr[32];
    if (wid < 4) {
#pragma unroll
      for (int T = 0; T < 2; ++T)
#pragma unroll
        for (int s = 0; s < 16; ++s) fr[T * 16 + s] = *(LDSP u32x4*)(smem + L_BIG + (32 * wid + 16 * T + n) * 1040 + s * 64 + g * 16);
    } else {
#pragma unroll
      for (int grp = 0; grp < 2; ++grp)
#pragma unroll
        for (int s = 0; s < 4; ++s) {
          u32x2 r[8];
#pragma unroll
          for (int e = 0; e < 8; ++e) r[e] = *(LDSP u32x2*)(smem + L_BIG + (32 * s + 8 * g + e) * 1040 + (128 * (wid - 4) + 64 * grp + 4 * n) * 2);
#pragma unroll
          for (int tt = 0; tt < 4; ++tt) {
            const u32 sel = (tt & 1) ? 0x07060302u : 0x05040100u;
            u32x4 f = {__builtin_amdgcn_perm(r[1][tt >> 1], r[0][tt >> 1], sel), __builtin_amdgcn_perm(r[3][tt >> 1], r[2][tt >> 1], sel),
                       __builtin_amdgcn_perm(r[5][tt >> 1], r[4][tt >> 1], sel), __builtin_amdgcn_perm(r[7][tt >> 1], r[6][tt >> 1], sel)};
            fr[(grp * 4 + s) * 4 + tt] = f;
          }
        }
    }
#pragma unroll
    for (int i = 0; i < 32; ++i) asm volatile("" : "+v"(fr[i]));
    STAMP(2);
    {
      u32 off[2]; u32x4 gb[2];
#pragma unroll
      for (int u = 0; u < 2; ++u) off[u] = OFF_B + ((8 * hg + wid) * 128 + lane + 64 * u) * 16;
      poll<2>(ws, off, tag, gb);
#pragma unroll
      for (int u = 0; u < 2; ++u) *(LDSP u32x2*)(smem + L_QA + wid * 1056 + (lane + 64 * u) * 8) = u32x2{gb[u][0], gb[u][2]};
    }
    STAMP(3);
    bar();
    float sc[8];
    if (wid < 4) {
      f32x4 a0 = {0, 0, 0, 0}, a1 = {0, 0, 0, 0};
#pragma unroll
      for (int s = 0; s < 16; ++s) {
        u32x4 B = *(LDSP u32x4*)(smem + L_QA + (n & 7) * 1056 + (32 * s + 8 * g) * 2);
        a0 = mfma(fr[s], B, a0);
        a1 = mfma(fr[16 + s], B, a1);
      }
      const float kscale = 0.08838834764831845f * 1.4426950408889634f;
      float m = -3.0e38f;
#pragma unroll
      for (int r = 0; r < 4; ++r) { sc[r] = a0[r] * kscale; sc[4 + r] = a1[r] * kscale; m = fmaxf(m, fmaxf(sc[r], sc[4 + r])); }
      m = xmax32(xmax16(m));
      if (lane < 8) *(LDSP float*)(smem + L_MX + (wid * 8 + lane) * 4) = m;
    } else if (t < 10) {
      srd_t s = mksrd(wo, 4096 * 640 * 2);
#pragma unroll
      for (int u = 0; u < 16; ++u) {
        const int blk = (wid - 4) * 16 + u;
        dma16(s, ((lane >> 3) * 640 + (lane & 7) * 8) * 2, ((512 * xc + 8 * blk) * 640 + 64 * t) * 2, lds0 + L_BIG + blk * 1024);
      }
    }
    STAMP(4);
    bar();
    float Mx = 0;
    if (wid < 4) {
      const int hd = n & 7;
      Mx = fmaxf(fmaxf(*(LDSP float*)(smem + L_MX + (0 * 8 + hd) * 4), *(LDSP float*)(smem + L_MX + (1 * 8 + hd) * 4)),
                 fmaxf(*(LDSP float*)(smem + L_MX + (2 * 8 + hd) * 4), *(LDSP float*)(smem + L_MX + (3 * 8 + hd) * 4)));
      u32 pk[4];
      float ls = 0;
#pragma unroll
      for (int r = 0; r < 4; ++r) {
        pk[r] = pack2(__builtin_amdgcn_exp2f(sc[2 * r] - Mx), __builtin_amdgcn_exp2f(sc[2 * r + 1] - Mx));
        ls += bflo(pk[r]) + bfhi(pk[r]);
      }
      if (n < 8) {
        *(LDSP u32x2*)(smem + L_P + n * 288 + (32 * wid + 4 * g) * 2) = u32x2{pk[0], pk[1]};
        *(LDSP u32x2*)(smem + L_P + n * 288 + (32 * wid + 16 + 4 * g) * 2) = u32x2{pk[2], pk[3]};
      }
      ls = xsum32(xsum16(ls));
      if (lane < 8) *(LDSP float*)(smem + L_MX + 128 + (wid * 8 + lane) * 4) = ls;
    }
    bar();
    STAMP(5);
    if (wid == 0) {
      if (lane < 8) {
        float ls = 0;
#pragma unroll
        for (int w = 0; w < 4; ++w) ls += *(LDSP float*)(smem + L_MX + 128 + (w * 8 + lane) * 4);
        st16c(ws, OFF_ML + ((8 * hg + lane) * 32 + ks) * 16, gran(__float_as_uint(Mx), __float_as_uint(ls), tag));
      }
    } else if (wid >= 4) {
      u32x4 pa[4];
#pragma unroll
      for (int s = 0; s < 4; ++s) pa[s] = *(LDSP u32x4*)(smem + L_P + (n & 7) * 288 + (32 * s + 8 * g) * 2);
#pragma unroll
      for (int grp = 0; grp < 2; ++grp) {
        f32x4 acc[4];
#pragma unroll
        for (int tt = 0; tt < 4; ++tt) acc[tt] = f32x4{0, 0, 0, 0};
#pragma unroll
        for (int s = 0; s < 4; ++s)
#pragma unroll
          for (int tt = 0; tt < 4; ++tt) acc[tt] = mfma(pa[s], fr[(grp * 4 + s) * 4 + tt], acc[tt]);
#pragma unroll
        for (int u = 0; u < 2; ++u) {
          const bool hi = g >= 2;
          float o0 = hi ? acc[0][u + 2] : acc[0][u], o1 = hi ? acc[1][u + 2] : acc[1][u];
          float o2 = hi ? acc[2][u + 2] : acc[2][u], o3 = hi ? acc[3][u + 2] : acc[3][u];
          const int hd = 4 * (g & 1) + u + 2 * (g >> 1);
          st16c(ws, OFF_C + ((((8 * hg + hd) * 4 + (wid - 4)) * 32 + ks) * 32 + 16 * grp + n) * 16, gran(pack2(o0, o1), pack2(o2, o3), tag));
        }
      }
    }
    STAMP(6);
    if (t < 10) {
      vm0bar();
      u32x4 wof[16];
      if (wid < 4) {
#pragma unroll
        for (int s = 0; s < 16; ++s) wof[s] = trfrag(smem + L_BIG + (32 * s + 8 * g + (n >> 2)) * 128 + (16 * wid + 4 * (n & 3)) * 2, 128);
#pragma unroll
        for (int s = 0; s < 16; ++s) asm volatile("" : "+v"(wof[s]));
      }
      STAMP(7);
      if (wid < 4) {
        u32 off[2]; u32x4 gd[2];
#pragma unroll
        for (int u = 0; u < 2; ++u) off[u] = OFF_D + (((xc * 4 + wid) * 4 + 2 * (lane >> 5) + u) * 32 + (lane & 31)) * 16;
        poll<2>(ws, off, tag, gd);
        float v0 = bflo(gd[0][0]) + bflo(gd[1][0]), v1 = bfhi(gd[0][0]) + bfhi(gd[1][0]);
        float v2 = bflo(gd[0][2]) + bflo(gd[1][2]), v3 = bfhi(gd[0][2]) + bfhi(gd[1][2]);
        swap32(v0, v2); swap32(v1, v3);
        *(LDSP u32*)(smem + L_CTX + (wid * 128 + 4 * (lane & 31) + 2 * (lane >> 5)) * 2) = pack2(v0 + v2, v1 + v3);
      }
      STAMP(8);
      bar();
      if (wid < 4) {
        f32x4 acc = {0, 0, 0, 0};
#pragma unroll
        for (int s = 0; s < 16; ++s) acc = mfma(*(LDSP u32x4*)(smem + L_CTX + (32 * s + 8 * g) * 2), wof[s], acc);
        u32x4 gr = gather4(acc[0], tag);
        if (lane < 16 && (lane & 3) == 0) st16c(ws, OFF_E + (xc * 160 + 16 * t + 4 * wid + (lane >> 2)) * 16, gr);
      }
      STAMP(9);
    } else if (t < 15 && wid == 0) {
      const int cg = 5 * xc + (t - 10), a = lane >> 2, gq = lane & 3;
      const float xv = bf2f(__builtin_amdgcn_raw_buffer_load_b16(rx, (16 * cg + 4 * gq + g) * 2, 0, 0));
      u32 off1[1]; u32x4 ge[1];
      off1[0] = OFF_E + ((a & 7) * 160 + 4 * cg + gq) * 16;
      poll<1>(ws, off1, tag, ge);
      const float e = a < 8 ? 1.0f : 0.0f;
      const float hv = rbf(xv + rbf(reduce_a(e * bflo(ge[0][0]), e * bfhi(ge[0][0]), e * bflo(ge[0][2]), e * bfhi(ge[0][2]))));
      STAMP(7);
      u32 off[7]; u32x4 gg[7];
#pragma unroll
      for (int u = 0; u < 7; ++u) { int pr = a + 16 * u; if (pr >= 108) pr = 0; off[u] = OFF_G + ((cg * 108 + pr) * 4 + gq) * 16; }
      poll<7>(ws, off, tag, gg);
      STAMP(8);
      float v0 = 0, v1 = 0, v2 = 0, v3 = 0;
#pragma unroll
      for (int u = 0; u < 7; ++u) {
        const float k = (a + 16 * u < 108) ? 1.0f : 0.0f;
        v0 += k * bflo(gg[u][0]); v1 += k * bfhi(gg[u][0]); v2 += k * bflo(gg[u][2]); v3 += k * bfhi(gg[u][2]);
      }
      const float dn = rbf(reduce_a(v0, v1, v2, v3));
      if ((lane & 12) == 0) out[16 * cg + 4 * gq + g] = (u16)pack2(hv + dn, hv + dn);
      if (blockIdx.x == 8 * 26 && lane == 0) __builtin_amdgcn_raw_buffer_store_b32(tag, ws, 0, 0, 16);
      STAMP(9);
    }
    STAMP(14);
    FLUSH();
  }
}

__global__ void tiny_w(unsigned char* wsp) { if (threadIdx.x == 0) __builtin_amdgcn_raw_buffer_store_b32((u32)__builtin_amdgcn_s_memrealtime(), mkrsrc(wsp + OFF_DBG, 64), 0, 0, 16); }
__global__ void tiny_s(unsigned char* wsp) { const u32 t0 = (u32)__builtin_amdgcn_s_memrealtime(); const u32 z_ = __builtin_amdgcn_readfirstlane(__builtin_amdgcn_raw_buffer_load_b32(mkrsrc(wsp + OFF_DBG, 64), 0, 0, 16));
    while ((u32)__builtin_amdgcn_s_memrealtime() - z_ < 6000u && (u32)__builtin_amdgcn_s_memrealtime() - t0 < 10000u) __builtin_amdgcn_s_sleep(2); }
extern "C" void kernel_launch(void* const* d_in, const int* in_sizes, int n_in, void* d_out, int out_size, void* d_ws,
                              size_t ws_size, hipStream_t stream) {
  tiny_w<<<1, 64, 0, stream>>>((unsigned char*)d_ws);
  mega<<<256, 512, 0, stream>>>((const u16*)d_in[0], (const u16*)d_in[1], (const u16*)d_in[2], (const u16*)d_in[3], (const u16*)d_in[4],
                                (const u16*)d_in[5], (const u16*)d_in[7], (const u16*)d_in[8], (const u16*)d_in[9], (u16*)d_out,
                                (unsigned char*)d_ws);
  tiny_s<<<1, 64, 0, stream>>>((unsigned char*)d_ws);
}
```

```cpp
#define CODE_BYTES 17216
#define NT 2
#define WQ_SPLIT 1
#define WO_PF 0
#define HOPA 1
#define CKV_NT 1
#define S2DOT 1
#define WK_LATE 0
#define XBAR 0
#define MLC_MODE 0
#define GU_MODE 1
#define GU_TOUCH 0
#define WO_TOUCH 0
#define ML1W 8
#define MLOV 1
#define MS_M 2
#define WO_EARLY 6
#define WO_Q 4
#define TB 1
#define T_DELAY 40
#define MS_B 2
#include <hip/hip_runtime.h>

typedef unsigned short u16;
typedef unsigned u32;
typedef __attribute__((ext_vector_type(4))) u32 u32x4;
typedef __attribute__((ext_vector_type(2))) u32 u32x2;
typedef __attribute__((ext_vector_type(4))) float f32x4;
typedef __attribute__((ext_vector_type(2))) float f32x2;
typedef __attribute__((ext_vector_type(8))) __bf16 bf16x8;
typedef __attribute__((ext_vector_type(2))) __bf16 bf16x2;
typedef __attribute__((ext_vector_type(4))) short s16x4;
typedef __amdgpu_buffer_rsrc_t rsrc_t;
#define DI __device__ __forceinline__
#define LDSP __attribute__((address_space(3)))

#define MASKED 1
#define GATE_A 4
#define GATE_B 2
#define GATE_C 2
#define GATE_D 2
#define GATE_E 4
#define GATE_G 0
constexpr int SPIN_MAX = 200000;
constexpr u32 OFF_A = 4096;
constexpr u32 OFF_B = OFF_A + 32 * 16 * 64 * 16;
constexpr u32 OFF_ML = OFF_B + 32 * 128 * 16;
constexpr u32 OFF_D = OFF_ML + 32 * 32 * 16;
constexpr u32 OFF_E = OFF_D + 8 * 16 * 32 * 16;
constexpr u32 OFF_G = OFF_E + 8 * 160 * 16;
constexpr u32 OFF_C = OFF_G + 40 * 108 * 4 * 16;
constexpr u32 WS_USED = OFF_C + 32 * 4 * 32 * 32 * 16;
constexpr u32 OFF_DBG = 8u << 20;

constexpr int KVS = 1056;
constexpr int L_BIG = 0;
constexpr int L_WUV = 0, L_WG = 32768, L_WU = 32768 + 20480;
constexpr int L_SM = 128 * KVS;
constexpr int L_QA = L_SM;
constexpr int L_P = L_QA + 8 * 1056;
constexpr int L_MX = L_P + 4096;
constexpr int L_CTX = L_MX + 256;
constexpr int L_RED = L_SM;
constexpr int L_RED4 = 73728;
constexpr int L_Q = L_RED + 4096;
constexpr int L_CC = L_Q + 256;
constexpr int L_H = L_CC + 256;
constexpr int L_GU = L_H + 1280;
constexpr int L_ST = L_GU + 512;
constexpr int L_XN = L_ST + 64;
constexpr int L_SCR = L_SM + 14336;
constexpr int L_WOX = L_SCR + 1024;
constexpr int L_TOTAL = L_WOX + 12288;
static_assert(L_TOTAL <= 163840, "lds");

DI float bf2f(u32 b) { return __uint_as_float(b << 16); }
DI float bfhi(u32 w) { return __uint_as_float(w & 0xffff0000u); }
DI float bflo(u32 w) { return __uint_as_float(w << 16); }
DI u32 pack2(float lo, float hi) { f32x2 v = {lo, hi}; return __builtin_bit_cast(u32, __builtin_convertvector(v, bf16x2)); }
DI float rbf(float f) { return bflo(pack2(f, f)); }
DI void bar() { asm volatile("s_waitcnt lgkmcnt(0)\n\ts_barrier" ::: "memory"); }
#define FENCE() do { asm volatile("" ::: "memory"); __builtin_amdgcn_sched_barrier(0); } while (0)
DI void vm0bar() { asm volatile("s_waitcnt vmcnt(0) lgkmcnt(0)\n\ts_barrier" ::: "memory"); }
template <int C> DI float dpp(float v) { return __builtin_bit_cast(float, __builtin_amdgcn_update_dpp(0, __builtin_bit_cast(int, v), C, 0xf, 0xf, true)); }
constexpr int DPP_X1 = 0xB1, DPP_X2 = 0x4E, DPP_ROR4 = 0x124, DPP_ROR8 = 0x128;
DI void swap32(float& a, float& b) { auto r = __builtin_amdgcn_permlane32_swap(__float_as_uint(a), __float_as_uint(b), false, false); const u32 r0 = r[0], r1 = r[1]; a = __uint_as_float(r0); b = __uint_as_float(r1); }
DI void swap16(float& a, float& b) { auto r = __builtin_amdgcn_permlane16_swap(__float_as_uint(a), __float_as_uint(b), false, false); const u32 r0 = r[0], r1 = r[1]; a = __uint_as_float(r0); b = __uint_as_float(r1); }
DI float xsum16(float v) { float a = v, b = v; swap16(a, b); return a + b; }
DI float xsum32(float v) { float a = v, b = v; swap32(a, b); return a + b; }
DI float xmax16(float v) { float a = v, b = v; swap16(a, b); return fmaxf(a, b); }
DI float xmax32(float v) { float a = v, b = v; swap32(a, b); return fmaxf(a, b); }
DI float wave_sum(float v) { v += dpp<DPP_X1>(v); v += dpp<DPP_X2>(v); v += dpp<DPP_ROR4>(v); v += dpp<DPP_ROR8>(v); return xsum32(xsum16(v)); }
DI float reduce_a(float v0, float v1, float v2, float v3) {
  swap32(v0, v2); float X = v0 + v2;
  swap32(v1, v3); float Y = v1 + v3;
  swap16(X, Y); float Z = X + Y;
  Z += dpp<DPP_ROR4>(Z); Z += dpp<DPP_ROR8>(Z);
  return Z;
}
DI float reduce_a8(float v0, float v1, float v2, float v3) {
  swap32(v0, v2); float X = v0 + v2;
  swap32(v1, v3); float Y = v1 + v3;
  swap16(X, Y); float Z = X + Y;
  Z += dpp<DPP_ROR8>(Z);
  return Z;
}
DI f32x4 mfma(u32x4 a, u32x4 b, f32x4 c) { return __builtin_amdgcn_mfma_f32_16x16x32_bf16(__builtin_bit_cast(bf16x8, a), __builtin_bit_cast(bf16x8, b), c, 0, 0, 0); }
DI float dot2(u32 a, u32 b, float c) { return __builtin_amdgcn_fdot2_f32_bf16(__builtin_bit_cast(bf16x2, a), __builtin_bit_cast(bf16x2, b), c, false); }
DI rsrc_t mkrsrc(const void* p, u32 bytes) { return __builtin_amdgcn_make_buffer_rsrc((void*)p, 0, bytes, 0x00020000); }
DI u32x4 ld16(rsrc_t r, u32 off) { return __builtin_amdgcn_raw_buffer_load_b128(r, off, 0, 0); }
DI u32x4 ld16n(rsrc_t r, u32 off) { return __builtin_amdgcn_raw_buffer_load_b128(r, off, 0, NT); }
DI u32x2 ld8n(rsrc_t r, u32 off) { return __builtin_amdgcn_raw_buffer_load_b64(r, off, 0, NT); }
DI u32x2 ld8(rsrc_t r, u32 off) { return __builtin_amdgcn_raw_buffer_load_b64(r, off, 0, 0); }
#define LDC_AUX 16
#define STC_AUX 16
DI u32x4 ld16c(rsrc_t r, u32 off) { return __builtin_amdgcn_raw_buffer_load_b128(r, off, 0, LDC_AUX); }
DI void st16c(rsrc_t r, u32 off, u32x4 v) { __builtin_amdgcn_raw_buffer_store_b128(v, r, off, 0, STC_AUX); }
typedef __attribute__((ext_vector_type(4))) int srd_t;
DI srd_t mksrd(const void* p, u32 bytes) {
  unsigned long long a = (unsigned long long)p;
  srd_t s = {(int)(u32)a, (int)(u32)(a >> 32), (int)bytes, 0x00020000};
  return s;
}
DI void dma16(srd_t srd, u32 voff, u32 soff, u32 lds) {
  u32 keep;
  asm volatile("s_nop 4\n\ts_mov_b32 %0, m0\n\ts_mov_b32 m0, %1\n\ts_nop 0\n\tbuffer_load_dwordx4 %2, %3, %4 offen lds\n\ts_mov_b32 m0, %0"
               : "=&s"(keep) : "s"(lds), "v"(voff), "s"(srd), "s"(soff) : "memory");
}
DI void dma16n(srd_t srd, u32 voff, u32 soff, u32 lds) {
  u32 keep;
  asm volatile("s_nop 4\n\ts_mov_b32 %0, m0\n\ts_mov_b32 m0, %1\n\ts_nop 0\n\tbuffer_load_dwordx4 %2, %3, %4 offen nt lds\n\ts_mov_b32 m0, %0"
               : "=&s"(keep) : "s"(lds), "v"(voff), "s"(srd), "s"(soff) : "memory");
}
#define TOUCH_NT 0
#define TOUCH_MOD ""
DI void touch(srd_t srd, u32 voff, u32 soff, u32 lds) {
  u32 keep;
  asm volatile("s_nop 4\n\ts_mov_b32 %0, m0\n\ts_mov_b32 m0, %1\n\ts_nop 0\n\tbuffer_load_dword %2, %3, %4 offen" TOUCH_MOD " lds\n\ts_mov_b32 m0, %0"
               : "=&s"(keep) : "s"(lds), "v"(voff), "s"(srd), "s"(soff) : "memory");
}
DI void touchn(srd_t srd, u32 voff, u32 lds) {
  u32 keep;
  asm volatile("s_nop 4\n\ts_mov_b32 %0, m0\n\ts_mov_b32 m0, %1\n\ts_nop 0\n\tbuffer_load_dword %2, %3, 0 offen nt lds\n\ts_mov_b32 m0, %0"
               : "=&s"(keep) : "s"(lds), "v"(voff), "s"(srd) : "memory");
}
DI void touchc(srd_t srd, u32 voff, u32 lds) {
  u32 keep;
  asm volatile("s_nop 4\n\ts_mov_b32 %0, m0\n\ts_mov_b32 m0, %1\n\ts_nop 0\n\tbuffer_load_dword %2, %3, 0 offen sc1 lds\n\ts_mov_b32 m0, %0"
               : "=&s"(keep) : "s"(lds), "v"(voff), "s"(srd) : "memory");
}
#define TLBW 0
#define CKV_HALF 0
DI u32x2 trread(const char* p) { return __builtin_bit_cast(u32x2, __builtin_amdgcn_ds_read_tr16_b64_v4i16((LDSP s16x4*)p)); }
DI u32x4 trfrag(const char* p, int rowstride) {
  u32x2 a = trread(p), b = trread(p + 4 * rowstride);
  u32x4 r = {a[0], a[1], b[0], b[1]};
  return r;
}
DI bool tagok(u32x4 g, u32 tag) { return g[1] == tag && g[3] == ~tag; }
#define GSLEEP 0
#define MSLEEP 0
constexpr u32 OOB = 0xfffffff0u;
template <int N, int NG, int MS = 0> DI void poll(rsrc_t ws, const u32 (&off)[N], u32 tag, u32x4 (&g)[N], bool gate) {
  int it = SPIN_MAX;
  bool more;
  if (NG > 0) {
#pragma nounroll
    do {
      u32x4 t[NG > 0 ? NG : 1];
      bool ok = false;
#pragma unroll
      for (int i = 0; i < NG; ++i) t[i] = ld16c(ws, gate ? off[i] : OOB);
#pragma unroll
      for (int i = 0; i < NG; ++i) ok |= tagok(t[i], tag);
      more = !__any(ok) && --it > 0;
      if (GSLEEP && more) __builtin_amdgcn_s_sleep(GSLEEP);
      asm volatile("" ::: "memory");
    } while (more);
  }
  u32 o[N];
#pragma unroll
  for (int i = 0; i < N; ++i) { o[i] = off[i]; g[i] = u32x4{0, 0, 0, 0}; }
#pragma nounroll
  do {
    u32x4 t[N];
    bool bad = false;
#pragma unroll
    for (int i = 0; i < N; ++i) t[i] = ld16c(ws, o[i]);
#pragma unroll
    for (int i = 0; i < N; ++i) {
      const bool ok = tagok(t[i], tag);
      g[i][0] = ok ? t[i][0] : g[i][0]; g[i][2] = ok ? t[i][2] : g[i][2];
      o[i] = ok ? OOB : o[i];
      bad |= o[i] != OOB;
    }
    more = __any(bad) && --it > 0;
    if (MS && more) __builtin_amdgcn_s_sleep(MS);
    asm volatile("" ::: "memory");
  } while (more);
}
#define PD 3
#define PGAP 6
#define PL 0
DI void dmac(srd_t srd, u32 voff, u32 lds) {
  u32 keep;
  asm volatile("s_mov_b32 %0, m0\n\ts_mov_b32 m0, %1\n\ts_nop 0\n\tbuffer_load_dwordx4 %2, %3, 0 offen sc1 lds\n\ts_mov_b32 m0, %0"
               : "=&s"(keep) : "s"(lds), "v"(voff), "s"(srd) : "memory");
}
template <int N, int NG, int D, bool ON, int MS = 0> DI void pollp(rsrc_t ws, srd_t wsd, const u32 (&off)[N], u32 tag, u32x4 (&g)[N], bool gate,
                                                       char* smem, u32 lds0, int slot, int lane) {
  if (!ON) { poll<N, NG, MS>(ws, off, tag, g, gate); return; }
  int it = SPIN_MAX;
  if (NG > 0) {
    bool more;
#pragma nounroll
    do {
      u32x4 t[NG > 0 ? NG : 1];
      bool ok = false;
#pragma unroll
      for (int i = 0; i < NG; ++i) t[i] = ld16c(ws, gate ? off[i] : OOB);
#pragma unroll
      for (int i = 0; i < NG; ++i) ok |= tagok(t[i], tag);
      more = !__any(ok) && --it > 0;
      asm volatile("" ::: "memory");
    } while (more);
  }
  u32 o[N];
#pragma unroll
  for (int i = 0; i < N; ++i) { o[i] = off[i]; g[i] = u32x4{0, 0, 0, 0}; }
#pragma unroll
  for (int d = 0; d < D; ++d) {
#pragma unroll
    for (int i = 0; i < N; ++i) dmac(wsd, o[i], lds0 + slot + (d * N + i) * 1024);
    if (d + 1 < D) __builtin_amdgcn_s_sleep(PGAP);
  }
#define POLL_STEP(d) if (D > d) { \
    asm volatile("s_waitcnt vmcnt(%0)" :: "n"((D - 1) * N) : "memory"); \
    u32x4 v[N]; \
    _Pragma("unroll") for (int i = 0; i < N; ++i) v[i] = *(LDSP u32x4*)(smem + slot + (d * N + i) * 1024 + lane * 16); \
    bool bad = false; \
    _Pragma("unroll") for (int i = 0; i < N; ++i) { \
      const bool ok = tagok(v[i], tag); \
      g[i][0] = ok ? v[i][0] : g[i][0]; g[i][2] = ok ? v[i][2] : g[i][2]; \
      o[i] = ok ? OOB : o[i]; \
      bad |= o[i] != OOB; \
    } \
    if (!(__any(bad) && --it > 0)) break; \
    _Pragma("unroll") for (int i = 0; i < N; ++i) dmac(wsd, o[i], lds0 + slot + (d * N + i) * 1024); }
#pragma nounroll
  for (;;) { POLL_STEP(0) POLL_STEP(1) POLL_STEP(2) POLL_STEP(3) }
}
#define PARGS smem, lds0
DI u32x4 gran(u32 p0, u32 p1, u32 tag) { u32x4 g = {p0, tag, p1, ~tag}; return g; }
DI u32x4 gather4(float v, u32 tag) {
  float v1 = dpp<0x55>(v), v2 = dpp<0xAA>(v), v3 = dpp<0xFF>(v), v0 = dpp<0x00>(v);
  return gran(pack2(v0, v1), pack2(v2, v3), tag);
}
DI float ln_std(float var) { return rbf(__builtin_amdgcn_sqrtf(rbf(rbf(var) + 1e-5f))); }
DI float rcp(float v) { return __builtin_amdgcn_rcpf(v); }
#define PIN(x) asm volatile("" : "+v"(x))
#define HPERM 0
#define WQ_AUX0 0
#define WQ_AUX1 0
#define GU_SPLIT 2
#define MS_D 0
#define MS_E 0
#define MS_G 0
#define EP 0
#define EP_N 4
#define EP_W 7
#define GL 0
#define DL 0
#define TB_C 5
#define PSPEC 0
#define BG_LATE 0
#define BG_N 10

#define STAMP(i)
#define FLUSH()

extern "C" __global__ __launch_bounds__(512, 2) void mega(const u16* x, const u16* ckv, const u16* wq,
                                               const u16* wuk, const u16* wuv, const u16* wo,
                                               const u16* wg, const u16* wu, const u16* wd,
                                               u16* out, unsigned char* wsp) {
  __shared__ __attribute__((aligned(16))) char smem[L_TOTAL];
  const int tid = threadIdx.x, lane = tid & 63;
  const int wid = __builtin_amdgcn_readfirstlane(tid >> 6);
  const int xc = blockIdx.x & 7, l = blockIdx.x >> 3;
  const int g = lane >> 4, n = lane & 15;
  const u32 lds0 = (u32)(size_t)smem;
  STAMP(0);
  asm volatile("" :: "s"(x), "s"(ckv), "s"(wq), "s"(wuk), "s"(wuv), "s"(wo), "s"(wg), "s"(wu), "s"(wd), "s"(out), "s"(wsp));
  rsrc_t ws = mkrsrc(wsp, WS_USED);
  rsrc_t rx = mkrsrc(x, 1280);
  srd_t wsd = mksrd(wsp, WS_USED);
  u32 epoch = *(const __attribute__((address_space(4))) u32*)wsp;
#define GET_TAG() asm volatile("" : "+s"(epoch)); const u32 tag = epoch + 1

  if (l < 16) {
    const int hh = (l >> 2) ^ HPERM, h = 4 * xc + hh, j = l & 3;
    rsrc_t rwq = mkrsrc(wq, 640 * 4096 * 2), rwuk = mkrsrc(wuk, 512 * 4096 * 2), rwd = mkrsrc(wd, 1728 * 640 * 2);
    u32x4 xa = ld16(rx, lane * 16);
    u32 xb = __builtin_amdgcn_raw_buffer_load_b32(rx, 1024 + lane * 4, 0, 0);
    FENCE();
    if (XBAR) { asm volatile("s_barrier" ::: "memory"); FENCE(); }
    u32 xr[5];
#pragma unroll
    for (int s = 0; s < 5; ++s) xr[s] = __builtin_amdgcn_raw_buffer_load_b16(rx, (160 * j + 20 * wid + 4 * s + g) * 2, 0, 0);
    FENCE();
    u32x4 wqf[5], wkf[4];
#pragma unroll
    for (int s = 0; s < 5; ++s) wqf[s] = ld16n(rwq, ((160 * j + 20 * wid + 4 * s + g) * 4096 + h * 128 + 8 * n) * 2);
#define LOAD_WK() _Pragma("unroll") for (int s = 0; s < 4; ++s) wkf[s] = ld16n(rwuk, ((128 * j + 16 * wid + 4 * g + s) * 4096 + h * 128 + 8 * n) * 2)
    if (WK_LATE == 0) { LOAD_WK(); }
    FENCE();
    if (TLBW & 4) {
      const int fg_ = xc + 8 * (l >> 2), f0_ = 64 * fg_ + 16 * (l & 3);
      if (wid == 7) touchn(mksrd(wuv, 512 * 4096 * 2), lane < 16 ? ((128 * j + 8 * lane) * 4096 + h * 128) * 2 : OOB, lds0 + L_SCR);
      if (wid == 6 && fg_ < 27) touchn(mksrd(wg, 640 * 1728 * 2), lane < 40 ? (16 * lane * 1728 + f0_) * 2 : OOB, lds0 + L_SCR);
      if (wid == 5 && fg_ < 27) touchn(mksrd(wu, 640 * 1728 * 2), lane < 40 ? (16 * lane * 1728 + f0_) * 2 : OOB, lds0 + L_SCR);
      if (wid == 4 && fg_ < 27) touchn(mksrd(wd, 1728 * 640 * 2), lane < 4 ? (f0_ + 4 * lane) * 640 * 2 : OOB, lds0 + L_SCR);
    }
    if ((TLBW & 8) && wid == 3) {
      const int fg_ = xc + 8 * (l >> 2);
      u32 o = OOB;
      if (lane < 4) o = OFF_C + ((h * 4 + j) * 32 + 8 * lane) * 512;
      else if (lane == 4) o = OFF_ML + h * 32 * 16;
      else if (lane == 5) o = OFF_D + ((xc * 4 + hh) * 4 + j) * 512;
      else if (lane < 14) o = OFF_E + (lane - 6) * 160 * 16;
      else if (lane < 54) o = OFF_G + ((lane - 14) * 108 + 4 * fg_) * 64;
      else if (lane == 54) o = OFF_B + (h * 128 + 32 * j) * 16;
      touchc(mksrd(wsp, WS_USED), o, lds0 + L_SCR);
    }
    FENCE();
    float sum = bflo(xb) + bfhi(xb);
#pragma unroll
    for (int i = 0; i < 4; ++i) sum += bflo(xa[i]) + bfhi(xa[i]);
    const float mean = wave_sum(sum) * (1.0f / 640.0f);
    float sq = (bflo(xb) - mean) * (bflo(xb) - mean) + (bfhi(xb) - mean) * (bfhi(xb) - mean);
#pragma unroll
    for (int i = 0; i < 4; ++i) sq += (bflo(xa[i]) - mean) * (bflo(xa[i]) - mean) + (bfhi(xa[i]) - mean) * (bfhi(xa[i]) - mean);
    const float xrs = rcp(ln_std(wave_sum(sq) * (1.0f / 640.0f))), xmean = rbf(mean);
    u32x4 A1[5];
    {
      const u32 msk = (n & 1) ? 0xffff0000u : 0x0000ffffu;
      const int r = (n & 7) >> 1;
#pragma unroll
      for (int s = 0; s < 5; ++s) {
        const float a = rbf(bf2f(xr[s]) - xmean) * xrs;
        const u32 ab = pack2(a, a) & msk;
        A1[s] = u32x4{r == 0 ? ab : 0u, r == 1 ? ab : 0u, r == 2 ? ab : 0u, r == 3 ? ab : 0u};
        PIN(A1[s]);
      }
    }
    STAMP(1);
    FENCE();
    if (WK_LATE == 1) { LOAD_WK(); }
    FENCE();
    {
      f32x4 acc = {0, 0, 0, 0};
#pragma unroll
      for (int s = 0; s < (HOPA ? 5 : 20); ++s) acc = mfma(A1[s], wqf[s], acc);
      if (g < 2) *(LDSP f32x4*)(smem + L_RED + wid * 512 + (8 * n + 4 * g) * 4) = acc;
    }
    bar();
    STAMP(2);
    GET_TAG();
    if (wid == 0) {
      f32x2 pv[8];
#pragma unroll
      for (int w = 0; w < 8; ++w) pv[w] = *(LDSP f32x2*)(smem + L_RED + w * 512 + lane * 8);
      FENCE();
      const f32x2 v = ((pv[0] + pv[1]) + (pv[2] + pv[3])) + ((pv[4] + pv[5]) + (pv[6] + pv[7]));
      st16c(ws, OFF_A + ((h * 16 + j) * 64 + lane) * 16, gran(__float_as_uint(v[0]), __float_as_uint(v[1]), tag));
      u32 off[3]; u32x4 gq[3];
#pragma unroll
      for (int i = 0; i < 3; ++i) off[i] = OFF_A + ((h * 16 + ((j + 1 + i) & 3)) * 64 + lane) * 16;
      pollp<3, 0, PD, (PL & 1) != 0>(ws, wsd, off, tag, gq, false, PARGS, 73728, lane);
      float q0 = v[0], q1 = v[1];
#pragma unroll
      for (int i = 0; i < 3; ++i) { q0 += __uint_as_float(gq[i][0]); q1 += __uint_as_float(gq[i][2]); }
      *(LDSP u32*)(smem + L_Q + lane * 4) = pack2(q0, q1);
    }
    bar();
    STAMP(4);
    {
      const u32x4 q8 = *(LDSP u32x4*)(smem + L_Q + n * 16);
      float v[4];
#pragma unroll
      for (int s = 0; s < 4; ++s) {
        float a = 0.0f;
#pragma unroll
        for (int i = 0; i < 4; ++i) { const u32 w_ = wkf[s][i], q_ = q8[i]; a = dot2(w_, q_, a); }
        v[s] = a;
      }
      float p0 = v[0] + dpp<DPP_X1>(v[0]), p1 = v[1] + dpp<DPP_X1>(v[1]), p2 = v[2] + dpp<DPP_X1>(v[2]), p3 = v[3] + dpp<DPP_X1>(v[3]);
      float r0 = (n & 1) ? p1 : p0, r1 = (n & 1) ? p3 : p2;
      r0 += dpp<DPP_X2>(r0); r1 += dpp<DPP_X2>(r1);
      float z = (n & 2) ? r1 : r0;
      z += dpp<DPP_ROR4>(z); z += dpp<DPP_ROR8>(z);
      const float z1 = dpp<0x55>(z), z2 = dpp<0xAA>(z), z3 = dpp<0xFF>(z);
      if (n == 0) st16c(ws, OFF_B + (h * 128 + 32 * j + 4 * wid + g) * 16, gran(pack2(z, z1), pack2(z2, z3), tag));
    }
    STAMP(5);
    const int fg = xc + 8 * (l >> 2);
    const bool ffn = fg < 27;
    const int f0 = 64 * fg + 16 * (l & 3);
    {
      srd_t s = mksrd(wuv, 512 * 4096 * 2);
#pragma unroll
      for (int u = 0; u < 4; ++u)
        dma16n(s, (g * 4096 + n * 8) * 2, ((128 * j + 16 * wid + 4 * u) * 4096 + h * 128) * 2, lds0 + L_WUV + (16 * wid + 4 * u) * 256);
    }
    if (WO_PF) {
      srd_t s = mksrd(wo, 4096 * 640 * 2);
#pragma unroll
      for (int u = 0; u < 5; ++u) dma16(s, lane * 16, 512 * 640 * 2 * xc + ((l * 8 + wid) * 5 + u) * 1024, lds0 + L_SCR);
    }
    if (GU_TOUCH && ffn && wid < 5) {
      srd_t s = mksrd((l & 2) ? wu : wg, 640 * 1728 * 2);
      touch(s, ((320 * (l & 1) + 64 * wid + lane) * 1728 + 64 * fg) * 2, 0, lds0 + L_SCR);
    }
    u32x2 wdr[8];
    if (ffn && wid < 5) {
#pragma unroll
      for (int e = 0; e < 8; ++e) wdr[e] = ld8n(rwd, ((f0 + 4 * g + (e & 3)) * 640 + 64 * (2 * wid + (e >> 2)) + 4 * n) * 2);
    }
#define GU_DMA(lo, hi) do { srd_t sg = mksrd(wg, 640 * 1728 * 2), su = mksrd(wu, 640 * 1728 * 2); \
      _Pragma("unroll") for (int u = (lo); u < (hi); ++u) { const int blk = u * 8 + wid;   \
        const u32 voff = ((lane >> 1) * 1728 + (lane & 1) * 8) * 2; \
        if (blk < 20) dma16n(sg, voff, (blk * 32 * 1728 + f0) * 2, lds0 + L_WG + blk * 1024); \
        else dma16n(su, voff, ((blk - 20) * 32 * 1728 + f0) * 2, lds0 + L_WU + (blk - 20) * 1024); } } while (0)
#define GU_OWN(lo, hi) do { if (wid < 4) { srd_t sm = mksrd((wid & 1) ? wu : wg, 640 * 1728 * 2); \
      _Pragma("unroll") for (int u = (lo); u < (hi); ++u) { const int blk = 10 * (wid >> 1) + u; \
        dma16n(sm, ((lane >> 1) * 1728 + (lane & 1) * 8) * 2, (blk * 32 * 1728 + f0) * 2, lds0 + ((wid & 1) ? L_WU : L_WG) + blk * 1024); } } } while (0)
    if (EP) { if (ffn) GU_OWN(0, EP_N); }
    else if (ffn) { if (GU_MODE == 0) GU_DMA(0, 5); else if (GU_MODE == 1) GU_DMA(0, GU_SPLIT); }
    const int eg = EP ? (wid & 3) : wid;
    u32x2 xres = ld8(rx, (32 * eg + (lane & 31)) * 8 < 1280 ? (32 * eg + (lane & 31)) * 8 : 0);
    const float xext = bf2f(__builtin_amdgcn_raw_buffer_load_b16(rx, (4 * (128 + 8 * (wid & 3) + (lane & 7)) + g) * 2, 0, 0));
    const float piv = bf2f(__builtin_amdgcn_raw_buffer_load_b16(rx, 0, 0, 0));
    STAMP(13);
    vm0bar();
    STAMP(3);
    u32x4 wvf[4];
#pragma unroll
    for (int s = 0; s < 4; ++s) { wvf[s] = trfrag(smem + L_WUV + (32 * s + 8 * g + (n >> 2)) * 256 + (16 * wid + 4 * (n & 3)) * 2, 256); PIN(wvf[s]); }
    u32x4 wdf[4];
#pragma unroll
    for (int t = 0; t < 4; ++t) {
      const u32 sel = (t & 1) ? 0x07060302u : 0x05040100u;
      wdf[t][0] = __builtin_amdgcn_perm(wdr[1][t >> 1], wdr[0][t >> 1], sel);
      wdf[t][1] = __builtin_amdgcn_perm(wdr[3][t >> 1], wdr[2][t >> 1], sel);
      wdf[t][2] = __builtin_amdgcn_perm(wdr[5][t >> 1], wdr[4][t >> 1], sel);
      wdf[t][3] = __builtin_amdgcn_perm(wdr[7][t >> 1], wdr[6][t >> 1], sel);
      PIN(wdf[t]);
    }
    STAMP(6);
    {
      const int a = lane >> 2, gq = lane & 3;
      u32 off[4]; u32x4 gm[4];
#pragma unroll
      for (int u = 0; u < 2; ++u) off[u] = OFF_ML + (h * 32 + a + 16 * u) * 16;
#pragma unroll
      for (int u = 0; u < 2; ++u) off[2 + u] = OFF_C + (((h * 4 + j) * 32 + a + 16 * u) * 32 + 4 * wid + gq) * 16;
      float sc0 = 0, sc1 = 0, rl = 0;
      if (ML1W) {
        if (wid == ML1W - 1) {
          u32 om[2] = {gq == 0 ? off[0] : OOB, gq == 0 ? off[1] : OOB};
          poll<2, 0, MS_M>(ws, om, tag, *(u32x4(*)[2])gm, false);
          if (gq == 0) {
            *(LDSP u32x2*)(smem + L_RED + a * 8) = u32x2{gm[0][0], gm[0][2]};
            *(LDSP u32x2*)(smem + L_RED + 128 + a * 8) = u32x2{gm[1][0], gm[1][2]};
          }
        }
        bar();
        STAMP(7);
        u32x4 c0, c1;
        if (MLOV) { c0 = ld16c(ws, off[2]); c1 = ld16c(ws, off[3]); FENCE(); }
        const u32x2 ml0 = *(LDSP u32x2*)(smem + L_RED + a * 8), ml1 = *(LDSP u32x2*)(smem + L_RED + 128 + a * 8);
        if (MLOV) {
          const float m0 = __uint_as_float(ml0[0]), m1 = __uint_as_float(ml1[0]);
          float M = fmaxf(m0, m1);
          M = fmaxf(M, dpp<DPP_ROR4>(M)); M = fmaxf(M, dpp<DPP_ROR8>(M)); M = xmax32(xmax16(M));
          sc0 = __builtin_amdgcn_exp2f(m0 - M); sc1 = __builtin_amdgcn_exp2f(m1 - M);
          float L = sc0 * __uint_as_float(ml0[1]) + sc1 * __uint_as_float(ml1[1]);
          L += dpp<DPP_ROR4>(L); L += dpp<DPP_ROR8>(L); L = xsum32(xsum16(L));
          rl = rcp(L);
          PIN(sc0); PIN(sc1); PIN(rl);
          FENCE();
          if (__any(!tagok(c0, tag) || !tagok(c1, tag))) poll<2, 0>(ws, *(u32(*)[2])(off + 2), tag, *(u32x4(*)[2])(gm + 2), false);
          else { gm[2] = c0; gm[3] = c1; }
        } else {
          poll<2, 0>(ws, *(u32(*)[2])(off + 2), tag, *(u32x4(*)[2])(gm + 2), false);
          gm[0][0] = ml0[0]; gm[0][2] = ml0[1]; gm[1][0] = ml1[0]; gm[1][2] = ml1[1];
        }
      } else if (MLC_MODE == 0) {
        pollp<2, GATE_C, PD, (PL & 4) != 0>(ws, wsd, *(u32(*)[2])off, tag, *(u32x4(*)[2])gm, gq == 0, PARGS, 73728 + wid * 6144, lane);
        STAMP(7);
        pollp<2, 0, PD, (PL & 8) != 0>(ws, wsd, *(u32(*)[2])(off + 2), tag, *(u32x4(*)[2])(gm + 2), false, PARGS, 73728 + wid * 6144, lane);
      } else {
        poll<4, GATE_C>(ws, off, tag, gm, gq == 0);
      }
      STAMP(8);
      if (!(ML1W && MLOV)) {
        const float m0 = __uint_as_float(gm[0][0]), m1 = __uint_as_float(gm[1][0]);
        float M = fmaxf(m0, m1);
        M = fmaxf(M, dpp<DPP_ROR4>(M)); M = fmaxf(M, dpp<DPP_ROR8>(M)); M = xmax32(xmax16(M));
        sc0 = __builtin_amdgcn_exp2f(m0 - M); sc1 = __builtin_amdgcn_exp2f(m1 - M);
        float L = sc0 * __uint_as_float(gm[0][2]) + sc1 * __uint_as_float(gm[1][2]);
        L += dpp<DPP_ROR4>(L); L += dpp<DPP_ROR8>(L); L = xsum32(xsum16(L));
        rl = rcp(L);
      }
      float z = reduce_a(sc0 * bflo(gm[2][0]) + sc1 * bflo(gm[3][0]), sc0 * bfhi(gm[2][0]) + sc1 * bfhi(gm[3][0]),
                         sc0 * bflo(gm[2][2]) + sc1 * bflo(gm[3][2]), sc0 * bfhi(gm[2][2]) + sc1 * bfhi(gm[3][2]));
      z *= rl;
      const int G = 4 * wid + gq;
      if ((lane & 12) == 0) *(LDSP u16*)(smem + L_CC + (64 * (G >> 4) + 16 * g + (G & 15)) * 2) = (u16)pack2(z, z);
    }
    bar();
    {
      u32x4 A[4];
#pragma unroll
      for (int s = 0; s < 4; ++s) A[s] = *(LDSP u32x4*)(smem + L_CC + (32 * s + 8 * g) * 2);
      FENCE();
      f32x4 acc = {0, 0, 0, 0};
      if (DL) {
        if (wid < 4) {
          f32x4 acc2 = {0, 0, 0, 0};
#pragma unroll
          for (int s = 0; s < 4; ++s) { acc = mfma(A[s], wvf[s], acc); acc2 = mfma(A[s], wvf[(DL ? 4 : 0) + s], acc2); }
          u32x4 gr = gather4(g == 0 ? acc[0] : acc2[0], tag);
          if (lane < 32 && (lane & 3) == 0) st16c(ws, OFF_D + (((xc * 4 + hh) * 4 + j) * 32 + 8 * wid + (lane >> 2)) * 16, gr);
        }
      } else {
#pragma unroll
        for (int s = 0; s < 4; ++s) acc = mfma(A[s], wvf[s], acc);
        u32x4 gr = gather4(acc[0], tag);
        if (lane < 16 && (lane & 3) == 0) st16c(ws, OFF_D + (((xc * 4 + hh) * 4 + j) * 32 + 4 * wid + (lane >> 2)) * 16, gr);
      }
    }
    STAMP(9);
    if (!ffn) { FLUSH(); return; }
    if (EP) { GU_OWN(EP_N, 10); if (wid < 4) asm volatile("s_waitcnt vmcnt(0)" ::: "memory"); }
    else {
    if (GU_MODE == 1) { GU_DMA(GU_SPLIT, 5); } else if (GU_MODE == 2) { GU_DMA(0, 5); }
    if (GU_MODE != 0) vm0bar();
    }
    u32x4 guf[10];
    const int mat = wid & 1, kh = (wid >> 1) & 1;
    if (wid < 4) {
      f32x4 cs = {0, 0, 0, 0};
      const u32x4 ones = {0x3f803f80u, 0x3f803f80u, 0x3f803f80u, 0x3f803f80u};
#pragma unroll
      for (int s = 0; s < 10; ++s) guf[s] = trfrag(smem + (mat ? L_WU : L_WG) + (32 * (10 * kh + s) + 8 * g + (n >> 2)) * 32 + 8 * (n & 3), 32);
      FENCE();
#pragma unroll
      for (int s = 0; s < 10; ++s) cs = mfma(guf[s], ones, cs);
      if (n == 0) *(LDSP f32x4*)(smem + L_GU + 256 + (mat * 2 + kh) * 64 + g * 16) = cs;
#pragma unroll
      for (int s = 0; s < 10; ++s) PIN(guf[s]);
    }
    STAMP(10);
    if (EP ? wid >= 4 : wid < 5) {
      constexpr int NE = EP ? 5 : 4;
      const int gi = EP ? wid - 4 : wid;
      u32 off[NE]; u32x4 ge[NE];
#pragma unroll
      for (int u = 0; u < 4; ++u) off[u] = OFF_E + ((4 * (lane >> 5) + u) * 160 + 32 * gi + (lane & 31)) * 16;
      if (EP) off[NE - 1] = OFF_E + ((lane >> 3) * 160 + 128 + 8 * gi + (lane & 7)) * 16;
      poll<NE, GATE_E, MS_E>(ws, off, tag, ge, (lane & 31) == 0);
      float v0 = (bflo(ge[0][0]) + bflo(ge[1][0])) + (bflo(ge[2][0]) + bflo(ge[3][0]));
      float v1 = (bfhi(ge[0][0]) + bfhi(ge[1][0])) + (bfhi(ge[2][0]) + bfhi(ge[3][0]));
      float v2 = (bflo(ge[0][2]) + bflo(ge[1][2])) + (bflo(ge[2][2]) + bflo(ge[3][2]));
      float v3 = (bfhi(ge[0][2]) + bfhi(ge[1][2])) + (bfhi(ge[2][2]) + bfhi(ge[3][2]));
      swap32(v0, v2); swap32(v1, v3);
      float a0 = rbf(v0 + v2), a1 = rbf(v1 + v3);
      u32 xw = lane < 32 ? xres[0] : xres[1];
      const u32 hw = pack2(bflo(xw) + a0, bfhi(xw) + a1);
      *(LDSP u32*)(smem + L_H + (32 * gi + (lane & 31)) * 8 + (lane >> 5) * 4) = hw;
      const float d0 = bflo(hw) - piv, d1 = bfhi(hw) - piv;
      float s1 = d0 + d1, s2 = d0 * d0 + d1 * d1;
      if (EP) {
        float z = reduce_a8(bflo(ge[NE - 1][0]), bfhi(ge[NE - 1][0]), bflo(ge[NE - 1][2]), bfhi(ge[NE - 1][2]));
        const float he = rbf(xext + rbf(z));
        if ((lane & 8) == 0) *(LDSP u16*)(smem + L_H + (4 * (128 + 8 * gi + (lane & 7)) + g) * 2) = (u16)(__float_as_uint(he) >> 16);
        const float de = (lane & 8) == 0 ? he - piv : 0.0f;
        s1 += de; s2 += de * de;
      }
      s1 += dpp<DPP_X1>(s1); s2 += dpp<DPP_X1>(s2); s1 += dpp<DPP_X2>(s1); s2 += dpp<DPP_X2>(s2);
      s1 += dpp<DPP_ROR4>(s1); s2 += dpp<DPP_ROR4>(s2); s1 += dpp<DPP_ROR8>(s1); s2 += dpp<DPP_ROR8>(s2);
      swap16(s1, s2);
      float t12 = s1 + s2;
      t12 = xsum32(t12);
      if (n == 0 && g < 2) *(LDSP float*)(smem + L_ST + gi * 8 + g * 4) = t12;
    }
    STAMP(11);
    bar();
    f32x4 acc = {0, 0, 0, 0};
    if (wid < 4) {
      u32x4 B[10];
#pragma unroll
      for (int s = 0; s < 10; ++s) B[s] = *(LDSP u32x4*)(smem + L_H + (32 * (10 * kh + s) + 8 * g) * 2);
      FENCE();
#pragma unroll
      for (int s = 0; s < 10; ++s) acc = mfma(guf[s], B[s], acc);
    }
    float mean2, rs2;
    {
      const f32x4 sa = *(LDSP f32x4*)(smem + L_ST), sb = *(LDSP f32x4*)(smem + L_ST + 16);
      const f32x2 sc = EP ? f32x2{0.0f, 0.0f} : *(LDSP f32x2*)(smem + L_ST + 32);
      const float S1 = ((sa[0] + sa[2]) + (sb[0] + sb[2]) + sc[0]) * (1.0f / 640.0f);
      const float S2 = ((sa[1] + sa[3]) + (sb[1] + sb[3]) + sc[1]) * (1.0f / 640.0f);
      mean2 = rbf(piv + S1);
      rs2 = rcp(ln_std(fmaxf(S2 - S1 * S1, 0.0f)));
      PIN(mean2); PIN(rs2);
    }
    if (wid < 4 && n == 0) *(LDSP f32x4*)(smem + L_GU + (mat * 2 + kh) * 64 + g * 16) = acc;
    bar();
    if (wid < 5) {
      f32x4 t[8];
#pragma unroll
      for (int i = 0; i < 8; ++i) t[i] = *(LDSP f32x4*)(smem + L_GU + i * 64 + g * 16);
      FENCE();
      const f32x4 ga = t[0] + t[1], ua = t[2] + t[3], gc = t[4] + t[5], uc = t[6] + t[7];
      float gv[4];
#pragma unroll
      for (int r = 0; r < 4; ++r) {
        float gt = rbf((ga[r] - mean2 * gc[r]) * rs2), up = rbf((ua[r] - mean2 * uc[r]) * rs2);
        gv[r] = (gt > 0.0f ? gt : 0.0f * gt) * up;
      }
      const u32 p01 = pack2(gv[0], gv[1]), p23 = pack2(gv[2], gv[3]);
      u32x4 A = n < 8 ? u32x4{p01, p23, 0, 0} : u32x4{0, 0, p01, p23};
      float o[4];
#pragma unroll
      for (int t2 = 0; t2 < 4; ++t2) { f32x4 z = {0, 0, 0, 0}; z = mfma(A, wdf[t2], z); o[t2] = z[0]; }
      if ((g & 1) == 0) {
        const int col4 = 16 * (2 * wid + (g >> 1)) + n;
        st16c(ws, OFF_G + (GL ? ((col4 >> 3) * 108 + 4 * fg + (l & 3)) * 8 + (col4 & 7) : ((col4 >> 2) * 108 + 4 * fg + (l & 3)) * 4 + (col4 & 3)) * 16, gran(pack2(o[0], o[1]), pack2(o[2], o[3]), tag));
      }
    }
    STAMP(12);
    STAMP(14);
    FLUSH();
  } else {
    const int t = l - 16, ks = 4 * xc + (t >> 2), hg = t & 3;
    if (wid < 2) {
      unsigned long long pc; u32 back;
      asm volatile("s_getpc_b64 %0\n.Lpf_%=:\n\ts_mov_b32 %1, .Lpf_%=-mega" : "=s"(pc), "=s"(back));
      srd_t s = mksrd((const void*)(pc - back), CODE_BYTES);
      dma16(s, lane * 16, (2 * t + wid) * 1024, lds0 + L_SCR);
    }
    if ((TLBW & 1) && wid == 2) touchn(mksrd(wo, 4096 * 640 * 2), t < 10 ? ((512 * xc + 8 * lane) * 640 + 64 * t) * 2 : OOB, lds0 + L_SCR);
    if ((TLBW & 2) && wid == 3) {
      u32 o = OOB;
      if (lane < 32) o = OFF_C + ((32 * hg + lane) * 32 + ks) * 512;
      else if (lane < 40) o = OFF_ML + ((8 * hg + lane - 32) * 32 + ks) * 16;
      else if (lane == 40) o = OFF_E + (xc * 160 + 16 * t) * 16;
      else if (lane < 45) o = OFF_D + (xc * 16 + 4 * (lane - 41)) * 512;
      else if (lane == 45) o = OFF_B + 8 * hg * 128 * 16;
      else if (lane == 46) o = OFF_B + (8 * hg + 4) * 128 * 16;
      touchc(mksrd(wsp, WS_USED), o, lds0 + L_SCR);
    }
    if (T_DELAY) __builtin_amdgcn_s_sleep(T_DELAY);
    {
      srd_t s = mksrd(ckv, 4096 * 512 * 2);
#pragma unroll
      for (int u = 0; u < (CKV_HALF ? 8 : 16); ++u) {
        if (CKV_NT) dma16n(s, lane * 16, (128 * ks + 16 * wid + u) * 1024, lds0 + L_BIG + (16 * wid + u) * KVS);
        else dma16(s, lane * 16, (128 * ks + 16 * wid + u) * 1024, lds0 + L_BIG + (16 * wid + u) * KVS);
      }
    }
    if (WO_TOUCH) {
      srd_t s = mksrd(wo, 4096 * 640 * 2);
      touch(s, t < 10 ? ((512 * xc + 64 * wid + lane) * 640 + 64 * t) * 2 : OOB, 0, lds0 + L_SCR);
    }
    GET_TAG();
    if (WO_TOUCH) asm volatile("s_waitcnt vmcnt(1) lgkmcnt(0)\n\ts_barrier" ::: "memory"); else vm0bar();
    STAMP(1);
    u32x4 fr[32];
#define V_FRAGS(c0, c1) do { _Pragma("unroll") for (int c = (c0); c < (c1); ++c) { _Pragma("unroll") for (int s = 0; s < 4; ++s) \
      fr[c * 4 + s] = trfrag(smem + L_BIG + (32 * s + 4 * g + (n >> 2)) * KVS + (128 * (wid - 4) + 16 * c + 4 * (n & 3)) * 2, 4 * KVS); } \
      _Pragma("unroll") for (int i = 4 * (c0); i < 4 * (c1); ++i) PIN(fr[i]); } while (0)
    if (wid < 4) {
#pragma unroll
      for (int T = 0; T < 2; ++T)
#pragma unroll
        for (int s = 0; s < 16; ++s) fr[T * 16 + s] = *(LDSP u32x4*)(smem + L_BIG + (32 * wid + 16 * T + n) * KVS + s * 64 + g * 16);
#pragma unroll
      for (int i = 0; i < 32; ++i) PIN(fr[i]);
    } else if (!TB) {
      V_FRAGS(0, 8);
    }
    STAMP(2);
    if (TB) {
      if (wid >= 4) {
        u32 off[4]; u32x4 gb[4];
#pragma unroll
        for (int u = 0; u < 4; ++u) off[u] = OFF_B + ((8 * hg + 2 * (wid - 4) + (u >> 1)) * 128 + lane + 64 * (u & 1)) * 16;
        poll<4, GATE_B * 2, MS_B>(ws, off, tag, gb, (lane & 31) == 0);
#pragma unroll
        for (int u = 0; u < 4; ++u) *(LDSP u32x2*)(smem + L_QA + (2 * (wid - 4) + (u >> 1)) * 1056 + (lane + 64 * (u & 1)) * 8) = u32x2{gb[u][0], gb[u][2]};
      }
    } else if (WO_EARLY == 2) {
      bar();
      if (wid < 4) {
        u32 off[4]; u32x4 gb[4];
#pragma unroll
        for (int u = 0; u < 4; ++u) off[u] = OFF_B + ((8 * hg + 2 * wid + (u >> 1)) * 128 + lane + 64 * (u & 1)) * 16;
        poll<4, GATE_B * 2>(ws, off, tag, gb, (lane & 31) == 0);
#pragma unroll
        for (int u = 0; u < 4; ++u) *(LDSP u32x2*)(smem + L_QA + (2 * wid + (u >> 1)) * 1056 + (lane + 64 * (u & 1)) * 8) = u32x2{gb[u][0], gb[u][2]};
      } else if (t < 10) {
        srd_t s = mksrd(wo, 4096 * 640 * 2);
#pragma unroll
        for (int u = 0; u < 16; ++u) {
          const int blk = (wid - 4) * 16 + u;
          dma16n(s, ((lane >> 3) * 640 + (lane & 7) * 8) * 2, ((512 * xc + 8 * blk) * 640 + 64 * t) * 2, lds0 + L_BIG + blk * 1024);
        }
      }
    } else {
      u32 off[2]; u32x4 gb[2];
#pragma unroll
      for (int u = 0; u < 2; ++u) off[u] = OFF_B + ((8 * hg + wid) * 128 + lane + 64 * u) * 16;
      if (PL & 2) bar();
      pollp<2, GATE_B, PD, (PL & 2) != 0, MS_B>(ws, wsd, off, tag, gb, (lane & 31) == 0, PARGS, 65536 + wid * 6144, lane);
#pragma unroll
      for (int u = 0; u < 2; ++u) *(LDSP u32x2*)(smem + L_QA + wid * 1056 + (lane + 64 * u) * 8) = u32x2{gb[u][0], gb[u][2]};
    }
    STAMP(3);
    bar();
    STAMP(4);
    float sc[8];
    if (wid < 4) {
      u32x4 B[16];
#pragma unroll
      for (int s = 0; s < 16; ++s) B[s] = *(LDSP u32x4*)(smem + L_QA + (n & 7) * 1056 + (32 * s + 8 * g) * 2);
      FENCE();
      f32x4 a0 = {0, 0, 0, 0}, a1 = {0, 0, 0, 0};
#pragma unroll
      for (int s = 0; s < 16; ++s) { a0 = mfma(fr[s], B[s], a0); a1 = mfma(fr[16 + s], B[s], a1); }
      const float kscale = 0.08838834764831845f * 1.4426950408889634f;
      float m = -3.0e38f;
#pragma unroll
      for (int r = 0; r < 4; ++r) { sc[r] = a0[r] * kscale; sc[4 + r] = a1[r] * kscale; m = fmaxf(m, fmaxf(sc[r], sc[4 + r])); }
      m = xmax32(xmax16(m));
      if (lane < 8) *(LDSP float*)(smem + L_MX + (lane * 4 + wid) * 4) = m;
    } else if (TB) {
      if (t < 10) {
        srd_t s = mksrd(wo, 4096 * 640 * 2);
#pragma unroll
        for (int u = 0; u < 3; ++u) {
          const int blk = (wid - 4) * 3 + u;
          dma16n(s, ((lane >> 3) * 640 + (lane & 7) * 8) * 2, ((512 * xc + 8 * blk) * 640 + 64 * t) * 2, lds0 + L_WOX + blk * 1024);
        }
      }
      V_FRAGS(0, TB_C);
    } else if (WO_EARLY == 6 && t < 10) {
      srd_t s = mksrd(wo, 4096 * 640 * 2);
#pragma unroll
      for (int u = 0; u < WO_Q; ++u) {
        const int blk = (wid - 4) * WO_Q + u;
        dma16n(s, ((lane >> 3) * 640 + (lane & 7) * 8) * 2, ((512 * xc + 8 * blk) * 640 + 64 * t) * 2, lds0 + L_BIG + blk * 1024);
      }
    } else if (WO_EARLY == 1 && t < 10) {
      srd_t s = mksrd(wo, 4096 * 640 * 2);
#pragma unroll
      for (int u = 0; u < 16; ++u) {
        const int blk = (wid - 4) * 16 + u;
        dma16n(s, ((lane >> 3) * 640 + (lane & 7) * 8) * 2, ((512 * xc + 8 * blk) * 640 + 64 * t) * 2, lds0 + L_BIG + blk * 1024);
      }
    }
    STAMP(5);
    bar();
    float Mx = 0;
    if (wid < 4) {
      const f32x4 m4 = *(LDSP f32x4*)(smem + L_MX + (n & 7) * 16);
      Mx = fmaxf(fmaxf(m4[0], m4[1]), fmaxf(m4[2], m4[3]));
      u32x4 pk;
      float ls = 0;
#pragma unroll
      for (int r = 0; r < 4; ++r) {
        pk[r] = pack2(__builtin_amdgcn_exp2f(sc[2 * r] - Mx), __builtin_amdgcn_exp2f(sc[2 * r + 1] - Mx));
        ls += bflo(pk[r]) + bfhi(pk[r]);
      }
      *(LDSP u32x4*)(smem + L_P + wid * 1024 + lane * 16) = pk;
      ls = xsum32(xsum16(ls));
      if (lane < 8) *(LDSP float*)(smem + L_MX + 128 + (lane * 4 + wid) * 4) = ls;
    } else if (TB) {
      V_FRAGS(TB_C, 8);
    }
    bar();
    STAMP(6);
#define WO_DMA() do { if (wid < 4) { srd_t s = mksrd(wo, 4096 * 640 * 2); \
      _Pragma("unroll") for (int u = 0; u < 16; ++u) { const int blk = wid * 16 + u; \
        dma16n(s, ((lane >> 3) * 640 + (lane & 7) * 8) * 2, ((512 * xc + 8 * blk) * 640 + 64 * t) * 2, lds0 + L_BIG + blk * 1024); } } } while (0)
    if (wid == 0) {
      if (lane < 8) {
        const f32x4 l4 = *(LDSP f32x4*)(smem + L_MX + 128 + lane * 16);
        st16c(ws, OFF_ML + ((8 * hg + lane) * 32 + ks) * 16, gran(__float_as_uint(Mx), __float_as_uint((l4[0] + l4[1]) + (l4[2] + l4[3])), tag));
      }
    } else if (wid >= 4) {
      u32x4 pa[4];
#pragma unroll
      for (int s = 0; s < 4; ++s) pa[s] = *(LDSP u32x4*)(smem + L_P + s * 1024 + lane * 16);
      FENCE();
#pragma unroll
      for (int grp = 0; grp < 2; ++grp) {
        f32x4 acc[4];
#pragma unroll
        for (int tt = 0; tt < 4; ++tt) acc[tt] = f32x4{0, 0, 0, 0};
#pragma unroll
        for (int s = 0; s < 4; ++s)
#pragma unroll
          for (int tt = 0; tt < 4; ++tt) acc[tt] = mfma(pa[s], fr[(grp * 4 + tt) * 4 + s], acc[tt]);
#pragma unroll
        for (int u = 0; u < 2; ++u) {
          const bool hi = g >= 2;
          float o0 = hi ? acc[0][u + 2] : acc[0][u], o1 = hi ? acc[1][u + 2] : acc[1][u];
          float o2 = hi ? acc[2][u + 2] : acc[2][u], o3 = hi ? acc[3][u + 2] : acc[3][u];
          const int hd = 4 * (g & 1) + u + 2 * (g >> 1);
          st16c(ws, OFF_C + ((((8 * hg + hd) * 4 + (wid - 4)) * 32 + ks) * 32 + 16 * grp + n) * 16, gran(pack2(o0, o1), pack2(o2, o3), tag));
        }
      }
    }
    if (WO_EARLY == 3 && t < 10) WO_DMA();
    STAMP(7);
    if (t < 10) {
      if (!WO_EARLY) {
        srd_t s = mksrd(wo, 4096 * 640 * 2);
#pragma unroll
        for (int u = 0; u < 8; ++u) {
          const int blk = wid * 8 + u;
          dma16n(s, ((lane >> 3) * 640 + (lane & 7) * 8) * 2, ((512 * xc + 8 * blk) * 640 + 64 * t) * 2, lds0 + L_BIG + blk * 1024);
        }
      }
      if (WO_EARLY == 4) { bar(); if (wid >= 4) { STAMP(11); } WO_DMA(); }
      if (WO_EARLY == 5) {
        bar(); if (wid >= 4) { STAMP(11); }
        srd_t s = mksrd(wo, 4096 * 640 * 2);
#pragma unroll
        for (int u = 0; u < 8; ++u) {
          const int blk = wid * 8 + u;
          dma16n(s, ((lane >> 3) * 640 + (lane & 7) * 8) * 2, ((512 * xc + 8 * blk) * 640 + 64 * t) * 2, lds0 + L_BIG + blk * 1024);
        }
      }
      if (TB) {
        bar();
        srd_t s = mksrd(wo, 4096 * 640 * 2);
#pragma unroll
        for (int u = 0; u < 7; ++u) {
          const int blk = 12 + wid + 8 * u;
          if (blk < 64) dma16n(s, ((lane >> 3) * 640 + (lane & 7) * 8) * 2, ((512 * xc + 8 * blk) * 640 + 64 * t) * 2, lds0 + L_BIG + blk * 1024);
        }
      } else if (WO_EARLY == 6) {
        bar();
        srd_t s = mksrd(wo, 4096 * 640 * 2);
#pragma unroll
        for (int u = 0; u < 8 - WO_Q / 2; ++u) {
          const int blk = 4 * WO_Q + wid * (8 - WO_Q / 2) + u;
          dma16n(s, ((lane >> 3) * 640 + (lane & 7) * 8) * 2, ((512 * xc + 8 * blk) * 640 + 64 * t) * 2, lds0 + L_BIG + blk * 1024);
        }
      }
      if (WO_EARLY == 3 || WO_EARLY == 4) { if (wid < 4) vm0bar(); else bar(); } else vm0bar();
      if (wid >= 4) { STAMP(12); }
      u32x4 wof[16];
      if (wid < 4) {
#pragma unroll
        for (int s = 0; s < 16; ++s) wof[s] = trfrag(smem + (TB && s < 3 ? L_WOX : L_BIG) + (32 * s + 8 * g + (n >> 2)) * 128 + (16 * wid + 4 * (n & 3)) * 2, 128);
#pragma unroll
        for (int s = 0; s < 16; ++s) PIN(wof[s]);
      }
      STAMP(8);
      if (wid < 4) {
        u32 off[2]; u32x4 gd[2];
#pragma unroll
        for (int u = 0; u < 2; ++u) off[u] = OFF_D + (((xc * 4 + wid) * 4 + 2 * (lane >> 5) + u) * 32 + (lane & 31)) * 16;
        pollp<2, GATE_D, PD, (PL & 16) != 0, MS_D>(ws, wsd, off, tag, gd, (lane & 31) == 0, PARGS, 65536 + wid * 6144, lane);
        float v0 = bflo(gd[0][0]) + bflo(gd[1][0]), v1 = bfhi(gd[0][0]) + bfhi(gd[1][0]);
        float v2 = bflo(gd[0][2]) + bflo(gd[1][2]), v3 = bfhi(gd[0][2]) + bfhi(gd[1][2]);
        swap32(v0, v2); swap32(v1, v3);
        *(LDSP u32*)(smem + L_CTX + (wid * 128 + 4 * (lane & 31) + 2 * (lane >> 5)) * 2) = pack2(v0 + v2, v1 + v3);
      }
      STAMP(9);
      bar();
      if (wid < 4) {
        u32x4 A[16];
#pragma unroll
        for (int s = 0; s < 16; ++s) A[s] = *(LDSP u32x4*)(smem + L_CTX + (32 * s + 8 * g) * 2);
        FENCE();
        f32x4 acc = {0, 0, 0, 0};
#pragma unroll
        for (int s = 0; s < 16; ++s) acc = mfma(A[s], wof[s], acc);
        u32x4 gr = gather4(acc[0], tag);
        if (lane < 16 && (lane & 3) == 0) st16c(ws, OFF_E + (xc * 160 + 16 * t + 4 * wid + (lane >> 2)) * 16, gr);
      }
      STAMP(10);
    } else if (t < 15 && wid == 0) {
      const int cg = 5 * xc + (t - 10), a = lane >> 2, gq = lane & 3;
      const float xv = bf2f(__builtin_amdgcn_raw_buffer_load_b16(rx, (16 * cg + 4 * gq + g) * 2, 0, 0));
      u32 off1[1]; u32x4 ge[1];
      off1[0] = OFF_E + ((a & 7) * 160 + 4 * cg + gq) * 16;
      poll<1, 0>(ws, off1, tag, ge, false);
      const float e = a < 8 ? 1.0f : 0.0f;
      float hv = rbf(xv + rbf(reduce_a(e * bflo(ge[0][0]), e * bfhi(ge[0][0]), e * bflo(ge[0][2]), e * bfhi(ge[0][2]))));
      PIN(hv);
      STAMP(11);
      u32 off[7]; u32x4 gg[7];
#pragma unroll
      for (int u = 0; u < 7; ++u) { int pr = a + 16 * u; if (pr >= 108) pr = 0; off[u] = OFF_G + (GL ? ((cg >> 1) * 108 + pr) * 8 + 4 * (cg & 1) + gq : (cg * 108 + pr) * 4 + gq) * 16; }
      pollp<7, GATE_G, PD, (PL & 64) != 0, MS_G>(ws, wsd, off, tag, gg, gq == 0, PARGS, 0, lane);
      STAMP(12);
      float v0 = 0, v1 = 0, v2 = 0, v3 = 0;
#pragma unroll
      for (int u = 0; u < 7; ++u) {
        const float k = (a + 16 * u < 108) ? 1.0f : 0.0f;
        v0 += k * bflo(gg[u][0]); v1 += k * bfhi(gg[u][0]); v2 += k * bflo(gg[u][2]); v3 += k * bfhi(gg[u][2]);
      }
      const float dn = rbf(reduce_a(v0, v1, v2, v3));
      if ((lane & 12) == 0) out[16 * cg + 4 * gq + g] = (u16)pack2(hv + dn, hv + dn);
      if (blockIdx.x == 8 * 26 && lane == 0) __builtin_amdgcn_raw_buffer_store_b32(tag, ws, 0, 0, 16);
      STAMP(13);
    }
    STAMP(14);
    FLUSH();
  }
}

extern "C" void kernel_launch(void* const* d_in, const int* in_sizes, int n_in, void* d_out, int out_size, void* d_ws,
                              size_t ws_size, hipStream_t stream) {
  mega<<<256, 512, 0, stream>>>((const u16*)d_in[0], (const u16*)d_in[1], (const u16*)d_in[2], (const u16*)d_in[3], (const u16*)d_in[4],
                                (const u16*)d_in[5], (const u16*)d_in[7], (const u16*)d_in[8], (const u16*)d_in[9], (u16*)d_out,
                                (unsigned char*)d_ws);
}
```
